```python
import torch

def _softmax(x, axis=-1):
    return torch.softmax(x, dim=axis)
def _gelu(x):
    return torch.nn.functional.gelu(x, approximate="tanh")
def _silu(x):
    return torch.nn.functional.silu(x)
def _sigmoid(x):
    return torch.sigmoid(x)
def _layernorm(x, axis=-1, eps=1e-5):
    m = x.mean(dim=axis, keepdim=True)
    v = x.var(dim=axis, keepdim=True, unbiased=False)
    return (x - m) / torch.sqrt(v + eps)
def _scatter_add(tgt, idx, src, axis=0):
    return tgt.clone().index_add_(axis, idx, src)
def _segment_sum(src, seg, n):
    out = torch.zeros((n,) + src.shape[1:], dtype=src.dtype)
    return out.index_add_(0, seg, src)
def _pad(x, pw):
    flat = tuple(v for pair in reversed(pw) for v in pair)
    return torch.nn.functional.pad(x, flat)

kernel_name = 'recurrent_scan__gqa_block_cached__dense_activation_gelu__decode'

def setup_inputs():

    _w = (lambda a: (lambda t: t.to(torch.bfloat16) if t.is_floating_point() else t)(torch.as_tensor(a)))
    def _A_setup():
        B, Sq, D = 8, 1, 512
        Hq, Hkv, Sk, Dh = 32, 8, 4096, 128
        assert Hq % Hkv == 0, (Hq, Hkv)
        return {
            "x": _w((lambda *s: torch.randn(*s, dtype=torch.bfloat16) * 0.05)(B, Sq, D)),
            "k_cache": _w((lambda *s: torch.randn(*s, dtype=torch.bfloat16) * 3.2)(B, Hkv, Sk, Dh)),
            "v_cache": _w((lambda *s: torch.randn(*s, dtype=torch.bfloat16) * 3.2)(B, Hkv, Sk, Dh)),
            "wq": _w((lambda *s: torch.randn(*s, dtype=torch.bfloat16) * 3.2)(D, Hq * Dh) * (D ** -0.5)),
            "wo": _w((lambda *s: torch.randn(*s, dtype=torch.bfloat16) * 3.2)(Hq * Dh, D) * ((Hq * Dh) ** -0.5)),
        }
    _A_in = _A_setup()

    def _B_setup():
        B, S, D, F = 8, 1, 512, 1792
        return {
            "x": _w((lambda *s: torch.randn(*s, dtype=torch.bfloat16) * 0.05)(B, S, D)),
            "w1": _w((lambda *s: torch.randn(*s, dtype=torch.bfloat16) * 3.2)(D, F) * (D ** -0.5)),
            "b1": _w((lambda *s: torch.randn(*s, dtype=torch.bfloat16) * 3.2)(F) * 0.01),
            "w2": _w((lambda *s: torch.randn(*s, dtype=torch.bfloat16) * 0.4)(F, D) * (F ** -0.5)),
            "b2": _w((lambda *s: torch.randn(*s, dtype=torch.bfloat16) * 0.4)(D) * 0.01),
        }
    _B_in = _B_setup()

    return {
        "A_x": _A_in["x"],
        "A_k_cache": _A_in["k_cache"],
        "A_v_cache": _A_in["v_cache"],
        "A_wq": _A_in["wq"],
        "A_wo": _A_in["wo"],
        "B_x": _B_in["x"],
        "B_w1": _B_in["w1"],
        "B_b1": _B_in["b1"],
        "B_w2": _B_in["w2"],
        "B_b2": _B_in["b2"],
    }

def reference(A_x, A_k_cache, A_v_cache, A_wq, A_wo, B_x, B_w1, B_b1, B_w2, B_b2):
    def _A_ref(x, k_cache, v_cache, wq, wo):
        B, Sq, D = x.shape
        Hq, Dh = 32, 128
        Hkv = k_cache.shape[1]
        rep = Hq // Hkv
        q = (lambda x, *d: x.permute(*d))((x @ wq).reshape(B, Sq, Hq, Dh), 0, 2, 1, 3)
        k = (lambda x, n, axis: torch.repeat_interleave(x, n, dim=axis))(k_cache, rep, axis=1)
        v = (lambda x, n, axis: torch.repeat_interleave(x, n, dim=axis))(v_cache, rep, axis=1)
        scores = torch.einsum("bhqd,bhkd->bhqk", q, k) * (Dh ** -0.5)
        p = _softmax(scores, axis=-1)
        o = torch.einsum("bhqk,bhkd->bhqd", p, v)
        return (lambda x, *d: x.permute(*d))(o, 0, 2, 1, 3).reshape(B, Sq, Hq * Dh) @ wo

    def _B_ref(x, w1, b1, w2, b2):
        h = torch.einsum("bsd,df->bsf", x, w1) + b1
        h = _gelu(h)
        return torch.einsum("bsf,fd->bsd", h, w2) + b2

    x = A_x
    xn = _layernorm(x, axis=-1)
    s = _A_ref(xn, A_k_cache, A_v_cache, A_wq, A_wo)
    g = _silu(_B_ref(xn, B_w1, B_b1, B_w2, B_b2))
    return x + s * g
```

```cpp
#include <hip/hip_runtime.h>
#include <hip/hip_bf16.h>
#include <cstdint>

typedef unsigned int u32;
typedef unsigned long long u64;
typedef unsigned short u16;
using u32x4 = __attribute__((ext_vector_type(4))) u32;
using f32x4 = __attribute__((ext_vector_type(4))) float;
using s16x8 = __attribute__((ext_vector_type(8))) short;
using bf16x2 = __attribute__((ext_vector_type(2))) __bf16;

constexpr int NB = 8, DM = 512, HQ = 32, HKV = 8, SK = 4096, DH = 128, FF = 1792;
constexpr int NWG = 256, NTHR = 512;
constexpr u32 TAG = 0x5A17C3E1u;
constexpr u32 SPIN_CAP = 1u << 20;

constexpr size_t G_Q = 0;
constexpr size_t G_H = G_Q + (size_t)NB * 2048;
constexpr size_t G_P = G_H + (size_t)NB * 896;
constexpr size_t G_S = G_P + (size_t)NB * HQ * 4 * 132;
constexpr size_t G_Y = G_S + (size_t)8 * NB * 32 * 64;
constexpr size_t G_END = G_Y + (size_t)8 * NB * 32 * 64;

__device__ __forceinline__ float bf16r(float x) { return (float)(__bf16)x; }
__device__ __forceinline__ u32 pack2(float lo, float hi) {
  bf16x2 v = {(__bf16)lo, (__bf16)hi};
  return __builtin_bit_cast(u32, v);
}
__device__ __forceinline__ float bflo(u32 x) { return __builtin_bit_cast(float, x << 16); }
__device__ __forceinline__ float bfhi(u32 x) { return __builtin_bit_cast(float, x & 0xffff0000u); }
__device__ __forceinline__ float bfu(u16 x) { return __builtin_bit_cast(float, (u32)x << 16); }
__device__ __forceinline__ float dot2(u32 k, u32 q, float acc) {
  return __builtin_amdgcn_fdot2_f32_bf16(__builtin_bit_cast(bf16x2, k), __builtin_bit_cast(bf16x2, q), acc, false);
}
template <int CTRL> __device__ __forceinline__ float dpp(float x) {
  return __builtin_bit_cast(float, __builtin_amdgcn_mov_dpp(__builtin_bit_cast(int, x), CTRL, 0xf, 0xf, true));
}
constexpr int XOR1 = 0xB1, XOR2 = 0x4E, XOR7 = 0x141, XOR8 = 0x128, ROR4 = 0x124;
__device__ __forceinline__ float xrow16_max(float x) {
  auto s = __builtin_amdgcn_permlane16_swap(__float_as_uint(x), __float_as_uint(x), false, false);
  x = fmaxf(__uint_as_float(s[0]), __uint_as_float(s[1]));
  auto t = __builtin_amdgcn_permlane32_swap(__float_as_uint(x), __float_as_uint(x), false, false);
  return fmaxf(__uint_as_float(t[0]), __uint_as_float(t[1]));
}
__device__ __forceinline__ float xrow16_sum(float x) {
  auto s = __builtin_amdgcn_permlane16_swap(__float_as_uint(x), __float_as_uint(x), false, false);
  x = __uint_as_float(s[0]) + __uint_as_float(s[1]);
  auto t = __builtin_amdgcn_permlane32_swap(__float_as_uint(x), __float_as_uint(x), false, false);
  return __uint_as_float(t[0]) + __uint_as_float(t[1]);
}
__device__ __forceinline__ void put_granule(u64* g, u32 value) {
  __hip_atomic_store(g, ((u64)TAG << 32) | (u64)value, __ATOMIC_RELAXED, __HIP_MEMORY_SCOPE_AGENT);
}
__device__ __forceinline__ u64 get_granule(const u64* g) {
  return __hip_atomic_load(g, __ATOMIC_RELAXED, __HIP_MEMORY_SCOPE_AGENT);
}
__device__ __forceinline__ void wg_barrier() {
  asm volatile("s_waitcnt lgkmcnt(0)" ::: "memory");
  __builtin_amdgcn_s_barrier();
  asm volatile("" ::: "memory");
}
__device__ __forceinline__ u32x4 ld_nt(const u16* p) {
  return __builtin_nontemporal_load(reinterpret_cast<const u32x4*>(p));
}
__device__ __forceinline__ u32x4 ld16(const u16* p) { return *reinterpret_cast<const u32x4*>(p); }

struct Params {
  const u16* x; const u16* kc; const u16* vc; const u16* wq; const u16* wo;
  const u16* w1; const u16* b1; const u16* w2; const u16* b2;
  u16* out; u64* ws;
};

constexpr int WROW = 144;
constexpr int L_WO = 0;
constexpr int L_W2 = L_WO + 128 * WROW;
constexpr int L_SVC = L_W2 + 64 * WROW;
constexpr int L_Q = L_SVC + 8 * 2 * 8 * 16 * 4;
constexpr int L_H = L_Q + 1024;
constexpr int L_O = L_H + 16 * 32 * 4;
constexpr int L_G = L_O + 16 * 64 * 4;
constexpr int L_FLAG = L_G + 256;
constexpr int L_P = L_FLAG + 16;
constexpr int L_MRG = L_P + 8 * 128 * 4 * 4;
constexpr int L_END = L_MRG + 8 * 4 * 132 * 4;

__global__ void __launch_bounds__(NTHR, 2) fused_decode_kernel(Params a) {
  __shared__ __attribute__((aligned(16))) unsigned char lds[L_END];
  const int w = blockIdx.x;
  const int tid = threadIdx.x, lane = tid & 63;
  const int v = __builtin_amdgcn_readfirstlane(tid >> 6);
  const int r = lane >> 4, c = lane & 15;
  const int bh = w >> 2, split = w & 3;
  const int ab = bh >> 3, ahkv = bh & 7;
  const int hi = w >> 3, cj = w & 7;
  u64* ws = a.ws;

  volatile u32* flag = reinterpret_cast<volatile u32*>(lds + L_FLAG);

  u32x4 xs[8];
  {
    const u16* xp = a.x + (lane >> 3) * DM + (lane & 7) * 64;
#pragma unroll
    for (int i = 0; i < 8; ++i) xs[i] = ld16(xp + 8 * i);
  }
  u32x4 xa[2];
#pragma unroll
  for (int s = 0; s < 2; ++s) xa[s] = ld16(a.x + (c & 7) * DM + 64 * v + 32 * s + 8 * r);
  s16x8 bq[2], bw[2];
  const bool h_on = (w < 224);
#pragma unroll
  for (int s = 0; s < 2; ++s) {
    const int k0 = 64 * v + 32 * s + 8 * r;
    const u16* pq = a.wq + (size_t)k0 * 4096 + 16 * w + c;
    const u16* ph = a.w1 + (size_t)k0 * FF + 8 * (h_on ? w : 0) + (c & 7);
#pragma unroll
    for (int jj = 0; jj < 8; ++jj) {
      bq[s][jj] = (short)pq[(size_t)jj * 4096];
      bw[s][jj] = (short)ph[(size_t)jj * FF];
    }
  }
  u32x4 st0, st1, st2;
  {
    const int row = tid >> 3, seg = tid & 7;
    st0 = ld16(a.wo + (size_t)(128 * hi + row) * DM + 64 * cj + 8 * seg);
    st1 = ld16(a.wo + (size_t)(128 * hi + 64 + row) * DM + 64 * cj + 8 * seg);
    const int r2 = row < 56 ? row : 0;
    st2 = ld16(a.w2 + (size_t)(56 * hi + r2) * DM + 64 * cj + 8 * seg);
  }

  const size_t kvbase = ((size_t)bh * SK + split * 1024 + 128 * v) * DH + (size_t)r * DH + 8 * c;
  u32x4 kv[32];
  constexpr int KEARLY = 24;
  if (v != 0) {
#pragma unroll
    for (int i = 0; i < KEARLY; ++i) kv[i] = ld_nt(a.kc + kvbase + (size_t)i * 4 * DH);
  }

  float mean_bf, sd_bf;
  {
    float s = 0.f;
#pragma unroll
    for (int i = 0; i < 8; ++i)
#pragma unroll
      for (int k = 0; k < 4; ++k) s += bflo(xs[i][k]) + bfhi(xs[i][k]);
    s += dpp<XOR1>(s); s += dpp<XOR2>(s); s += dpp<XOR7>(s);
    const float mean = s * (1.0f / DM);
    float q2 = 0.f;
#pragma unroll
    for (int i = 0; i < 8; ++i)
#pragma unroll
      for (int k = 0; k < 4; ++k) {
        const float d0 = bflo(xs[i][k]) - mean, d1 = bfhi(xs[i][k]) - mean;
        q2 = fmaf(d0, d0, q2); q2 = fmaf(d1, d1, q2);
      }
    q2 += dpp<XOR1>(q2); q2 += dpp<XOR2>(q2); q2 += dpp<XOR7>(q2);
    const float var_bf = bf16r(q2 * (1.0f / DM));
    const float m_l = bf16r(mean);
    const float s_l = bf16r(sqrtf(bf16r(var_bf + 1e-5f)));
    mean_bf = __shfl(m_l, 8 * (c & 7), 64);
    sd_bf = __shfl(s_l, 8 * (c & 7), 64);
  }
  {
    f32x4 accq = {0.f, 0.f, 0.f, 0.f}, acch = {0.f, 0.f, 0.f, 0.f};
#pragma unroll
    for (int s = 0; s < 2; ++s) {
      u32x4 af;
#pragma unroll
      for (int k = 0; k < 4; ++k) {
        const float lo = bf16r(bf16r(bflo(xa[s][k]) - mean_bf) / sd_bf);
        const float hi2 = bf16r(bf16r(bfhi(xa[s][k]) - mean_bf) / sd_bf);
        af[k] = (c < 8) ? pack2(lo, hi2) : 0u;
      }
      const s16x8 afr = __builtin_bit_cast(s16x8, af);
      s16x8 bws = bw[s];
      if (c >= 8) bws = s16x8{0, 0, 0, 0, 0, 0, 0, 0};
      accq = __builtin_amdgcn_mfma_f32_16x16x32_bf16(afr, bq[s], accq, 0, 0, 0);
      acch = __builtin_amdgcn_mfma_f32_16x16x32_bf16(afr, bws, acch, 0, 0, 0);
    }
    float* svc = reinterpret_cast<float*>(lds + L_SVC) + v * 256;
    if (lane < 32) {
#pragma unroll
      for (int k = 0; k < 4; ++k) {
        svc[(4 * r + k) * 16 + c] = accq[k];
        svc[128 + (4 * r + k) * 16 + c] = acch[k];
      }
    }
  }
  {
    const int row = tid >> 3, seg = tid & 7;
    *reinterpret_cast<u32x4*>(lds + L_WO + row * WROW + 16 * seg) = st0;
    *reinterpret_cast<u32x4*>(lds + L_WO + (64 + row) * WROW + 16 * seg) = st1;
    *reinterpret_cast<u32x4*>(lds + L_W2 + row * WROW + 16 * seg) = row < 56 ? st2 : u32x4{0u, 0u, 0u, 0u};
  }
  if (tid == 0) *flag = 0u;
  if (tid < 256) reinterpret_cast<u32*>(lds + L_H)[256 + tid] = 0u;
  reinterpret_cast<u32*>(lds + L_O)[512 + tid] = 0u;
  wg_barrier();

  if (v == 0) {
    const float* svc = reinterpret_cast<const float*>(lds + L_SVC);
    {
      const int b = lane >> 3, cp = lane & 7;
      float s0 = 0.f, s1 = 0.f;
#pragma unroll
      for (int u = 0; u < 8; ++u) { s0 += svc[u * 256 + b * 16 + 2 * cp]; s1 += svc[u * 256 + b * 16 + 2 * cp + 1]; }
      put_granule(ws + G_Q + (size_t)b * 2048 + 8 * w + cp, pack2(s0, s1));
    }
    if (h_on && lane < 32) {
      const int b = lane >> 2, cp = lane & 3;
      float hv[2];
#pragma unroll
      for (int e = 0; e < 2; ++e) {
        float s0 = 0.f;
#pragma unroll
        for (int u = 0; u < 8; ++u) s0 += svc[u * 256 + 128 + b * 16 + 2 * cp + e];
        const float t = bf16r(bf16r(s0) + bfu(a.b1[8 * w + 2 * cp + e]));
        const float inner = 0.7978845608028654f * (t + 0.044715f * t * t * t);
        hv[e] = 0.5f * t * (1.0f + tanhf(inner));
      }
      put_granule(ws + G_H + (size_t)b * 896 + 4 * w + cp, pack2(hv[0], hv[1]));
    }
    {
      const u64* qg = ws + G_Q + (size_t)ab * 2048 + ahkv * 256 + lane;
      u32 val[4];
      for (u32 spins = 0; spins < SPIN_CAP; ++spins) {
        bool ok = true;
#pragma unroll
        for (int k = 0; k < 4; ++k) { const u64 g = get_granule(qg + 64 * k); val[k] = (u32)g; ok &= (u32)(g >> 32) == TAG; }
        if (__all(ok)) break;
        __builtin_amdgcn_s_sleep(1);
      }
      u32* qs = reinterpret_cast<u32*>(lds + L_Q);
#pragma unroll
      for (int k = 0; k < 4; ++k) qs[64 * k + lane] = val[k];
      asm volatile("s_waitcnt lgkmcnt(0)" ::: "memory");
      if (lane == 0) *flag = 1u;
    }
    {
      u32 val[4];
      int idx[4];
#pragma unroll
      for (int k = 0; k < 4; ++k) idx[k] = min(lane + 64 * k, 223);
      for (u32 spins = 0; spins < SPIN_CAP; ++spins) {
        bool ok = true;
#pragma unroll
        for (int k = 0; k < 4; ++k) {
          const int b = idx[k] / 28, t = idx[k] % 28;
          const u64 g = get_granule(ws + G_H + (size_t)b * 896 + 28 * hi + t);
          val[k] = (u32)g; ok &= (u32)(g >> 32) == TAG;
        }
        if (__all(ok)) break;
        __builtin_amdgcn_s_sleep(1);
      }
      u32* hs = reinterpret_cast<u32*>(lds + L_H);
#pragma unroll
      for (int k = 0; k < 4; ++k) if (lane + 64 * k < 224) hs[(idx[k] / 28) * 32 + (idx[k] % 28)] = val[k];
      if (lane < 32) hs[(lane >> 2) * 32 + 28 + (lane & 3)] = 0u;
      asm volatile("s_waitcnt lgkmcnt(0)" ::: "memory");
#pragma unroll
      for (int ct = 0; ct < 4; ++ct) {
        f32x4 acc = {0.f, 0.f, 0.f, 0.f};
#pragma unroll
        for (int s = 0; s < 2; ++s) {
          const u32x4 af = *reinterpret_cast<const u32x4*>(lds + L_H + c * 128 + (32 * s + 8 * r) * 2);
          s16x8 bf;
#pragma unroll
          for (int jj = 0; jj < 8; ++jj)
            bf[jj] = *reinterpret_cast<const short*>(lds + L_W2 + (32 * s + 8 * r + jj) * WROW + (16 * ct + c) * 2);
          acc = __builtin_amdgcn_mfma_f32_16x16x32_bf16(__builtin_bit_cast(s16x8, af), bf, acc, 0, 0, 0);
        }
        if (lane < 32) {
#pragma unroll
          for (int k = 0; k < 4; ++k)
            put_granule(ws + G_Y + (((size_t)cj * NB + (4 * r + k)) * 32 + hi) * 64 + 16 * ct + c, __float_as_uint(acc[k]));
        }
      }
    }
#pragma unroll
    for (int i = 0; i < 32; ++i) kv[i] = ld_nt(a.kc + kvbase + (size_t)i * 4 * DH);
  } else {
#pragma unroll
    for (int i = KEARLY; i < 32; ++i) kv[i] = ld_nt(a.kc + kvbase + (size_t)i * 4 * DH);
    for (u32 spins = 0; spins < SPIN_CAP * 4u; ++spins) {
      if (*flag != 0u) break;
      __builtin_amdgcn_s_sleep(1);
    }
    asm volatile("" ::: "memory");
  }

  u32x4 qpk[4];
#pragma unroll
  for (int g = 0; g < 4; ++g) qpk[g] = *reinterpret_cast<const u32x4*>(lds + L_Q + g * 256 + c * 16);
  float sc[8];
#pragma unroll
  for (int grp = 0; grp < 8; ++grp) {
    float x[16];
#pragma unroll
    for (int t = 0; t < 4; ++t)
#pragma unroll
      for (int g = 0; g < 4; ++g) {
        float d = 0.f;
#pragma unroll
        for (int k = 0; k < 4; ++k) d = dot2(kv[4 * grp + t][k], qpk[g][k], d);
        x[t * 4 + g] = d;
      }
#define TR_STEP(HALF, CTRL, BIT) _Pragma("unroll") for (int h = 0; h < (HALF); ++h) { \
      const float lo_ = x[h], hi_ = x[h + (HALF)]; const float t1_ = lo_ + dpp<CTRL>(lo_), t2_ = hi_ + dpp<CTRL>(hi_); \
      x[h] = (c & (BIT)) ? t2_ : t1_; }
    TR_STEP(8, XOR8, 8) TR_STEP(4, XOR7, 4) TR_STEP(2, XOR2, 2) TR_STEP(1, XOR1, 1)
#undef TR_STEP
    sc[grp] = bf16r(bf16r(x[0]) * 0.08838834764831845f);
#pragma unroll
    for (int t = 0; t < 4; ++t) kv[4 * grp + t] = ld_nt(a.vc + kvbase + (size_t)(4 * grp + t) * 4 * DH);
  }
  float m = sc[0];
#pragma unroll
  for (int grp = 1; grp < 8; ++grp) m = fmaxf(m, sc[grp]);
  m = fmaxf(m, dpp<ROR4>(m)); m = fmaxf(m, dpp<XOR8>(m)); m = xrow16_max(m);
  float l = 0.f;
  {
    float* pl = reinterpret_cast<float*>(lds + L_P) + v * 512;
#pragma unroll
    for (int grp = 0; grp < 8; ++grp) {
      const float p = __builtin_amdgcn_exp2f((sc[grp] - m) * 1.4426950408889634f);
      l += p;
      pl[(16 * grp + 4 * (c >> 2) + r) * 4 + (c & 3)] = p;
    }
  }
  l += dpp<ROR4>(l); l += dpp<XOR8>(l); l = xrow16_sum(l);
  asm volatile("s_waitcnt lgkmcnt(0)" ::: "memory");

  float acc[32];
#pragma unroll
  for (int k = 0; k < 32; ++k) acc[k] = 0.f;
  {
    const f32x4* pl4 = reinterpret_cast<const f32x4*>(lds + L_P) + v * 128;
#pragma unroll
    for (int i = 0; i < 32; ++i) {
      const f32x4 p = pl4[4 * i + r];
      float vf[8];
#pragma unroll
      for (int k = 0; k < 4; ++k) { vf[2 * k] = bflo(kv[i][k]); vf[2 * k + 1] = bfhi(kv[i][k]); }
#pragma unroll
      for (int g = 0; g < 4; ++g)
#pragma unroll
        for (int k = 0; k < 8; ++k) acc[g * 8 + k] = fmaf(p[g], vf[k], acc[g * 8 + k]);
    }
  }
#pragma unroll
  for (int k = 0; k < 32; ++k) acc[k] = xrow16_sum(acc[k]);
  {
    float* mrg = reinterpret_cast<float*>(lds + L_MRG) + v * 4 * 132;
    if (r == 0) {
#pragma unroll
      for (int g = 0; g < 4; ++g) {
        *reinterpret_cast<f32x4*>(mrg + g * 132 + 4 + 8 * c) = f32x4{acc[g * 8], acc[g * 8 + 1], acc[g * 8 + 2], acc[g * 8 + 3]};
        *reinterpret_cast<f32x4*>(mrg + g * 132 + 4 + 8 * c + 4) = f32x4{acc[g * 8 + 4], acc[g * 8 + 5], acc[g * 8 + 6], acc[g * 8 + 7]};
      }
      if (c < 4) { mrg[c * 132] = m; mrg[c * 132 + 1] = l; }
    }
  }
  if (hi < 8 && v == 7) {
    const u64* yg = ws + G_Y + ((size_t)cj * NB + hi) * 32 * 64 + lane;
    float ysum = 0.f;
    for (u32 spins = 0; spins < SPIN_CAP; ++spins) {
      bool ok = true; ysum = 0.f;
#pragma unroll
      for (int i = 0; i < 32; ++i) { const u64 g = get_granule(yg + 64 * i); ysum += __uint_as_float((u32)g); ok &= (u32)(g >> 32) == TAG; }
      if (__all(ok)) break;
      __builtin_amdgcn_s_sleep(2);
    }
    const float y = bf16r(bf16r(ysum) + bfu(a.b2[64 * cj + lane]));
    const float gate = bf16r(y / (1.0f + __expf(-y)));
    reinterpret_cast<float*>(lds + L_G)[lane] = gate;
  }
  wg_barrier();

  {
    const int g = tid >> 7, d = tid & 127;
    const float* mrg = reinterpret_cast<const float*>(lds + L_MRG) + g * 132;
    float M = mrg[0];
#pragma unroll
    for (int u = 1; u < 8; ++u) M = fmaxf(M, mrg[u * 4 * 132]);
    float L = 0.f, o = 0.f;
#pragma unroll
    for (int u = 0; u < 8; ++u) {
      const float wt = __builtin_amdgcn_exp2f((mrg[u * 4 * 132] - M) * 1.4426950408889634f);
      L = fmaf(wt, mrg[u * 4 * 132 + 1], L);
      o = fmaf(wt, mrg[u * 4 * 132 + 4 + d], o);
    }
    u64* pg = ws + G_P + (((size_t)ab * HQ + ahkv * 4 + g) * 4 + split) * 132;
    put_granule(pg + d, __float_as_uint(o));
    if (d == 0) { put_granule(pg + 128, __float_as_uint(M)); put_granule(pg + 129, __float_as_uint(L)); }
  }

  {
    const u64* pg = ws + G_P + ((size_t)v * HQ + hi) * 4 * 132;
    u32 o0[4], o1[4], mm[4], ll[4];
    for (u32 spins = 0; spins < SPIN_CAP; ++spins) {
      bool ok = true;
#pragma unroll
      for (int s = 0; s < 4; ++s) {
        const u64 g0 = get_granule(pg + s * 132 + 2 * lane), g1 = get_granule(pg + s * 132 + 2 * lane + 1);
        const u64 gm = get_granule(pg + s * 132 + 128), gl = get_granule(pg + s * 132 + 129);
        o0[s] = (u32)g0; o1[s] = (u32)g1; mm[s] = (u32)gm; ll[s] = (u32)gl;
        ok &= ((u32)(g0 >> 32) == TAG) & ((u32)(g1 >> 32) == TAG) & ((u32)(gm >> 32) == TAG) & ((u32)(gl >> 32) == TAG);
      }
      if (__all(ok)) break;
      __builtin_amdgcn_s_sleep(2);
    }
    float M = __uint_as_float(mm[0]);
#pragma unroll
    for (int s = 1; s < 4; ++s) M = fmaxf(M, __uint_as_float(mm[s]));
    float L = 0.f, a0 = 0.f, a1 = 0.f;
#pragma unroll
    for (int s = 0; s < 4; ++s) {
      const float wt = __builtin_amdgcn_exp2f((__uint_as_float(mm[s]) - M) * 1.4426950408889634f);
      L = fmaf(wt, __uint_as_float(ll[s]), L);
      a0 = fmaf(wt, __uint_as_float(o0[s]), a0);
      a1 = fmaf(wt, __uint_as_float(o1[s]), a1);
    }
    const float inv = 1.0f / L;
    reinterpret_cast<u32*>(lds + L_O)[v * 64 + lane] = pack2(a0 * inv, a1 * inv);
  }
  wg_barrier();
  if (v < 4) {
    const int ct = v;
    f32x4 acc4 = {0.f, 0.f, 0.f, 0.f};
#pragma unroll
    for (int s = 0; s < 4; ++s) {
      const u32x4 af = *reinterpret_cast<const u32x4*>(lds + L_O + c * 256 + (32 * s + 8 * r) * 2);
      s16x8 bf;
#pragma unroll
      for (int jj = 0; jj < 8; ++jj)
        bf[jj] = *reinterpret_cast<const short*>(lds + L_WO + (32 * s + 8 * r + jj) * WROW + (16 * ct + c) * 2);
      acc4 = __builtin_amdgcn_mfma_f32_16x16x32_bf16(__builtin_bit_cast(s16x8, af), bf, acc4, 0, 0, 0);
    }
    if (lane < 32) {
#pragma unroll
      for (int k = 0; k < 4; ++k)
        put_granule(ws + G_S + (((size_t)cj * NB + (4 * r + k)) * 32 + hi) * 64 + 16 * ct + c, __float_as_uint(acc4[k]));
    }
  }
  if (hi < 8 && v == 4) {
    const u64* sg = ws + G_S + ((size_t)cj * NB + hi) * 32 * 64 + lane;
    float ssum = 0.f;
    for (u32 spins = 0; spins < SPIN_CAP; ++spins) {
      bool ok = true; ssum = 0.f;
#pragma unroll
      for (int i = 0; i < 32; ++i) { const u64 g = get_granule(sg + 64 * i); ssum += __uint_as_float((u32)g); ok &= (u32)(g >> 32) == TAG; }
      if (__all(ok)) break;
      __builtin_amdgcn_s_sleep(2);
    }
    const float sb = bf16r(ssum);
    const float gate = reinterpret_cast<const float*>(lds + L_G)[lane];
    const float xo = bfu(a.x[hi * DM + 64 * cj + lane]);
    const float res = xo + bf16r(sb * gate);
    const __bf16 ob = (__bf16)res;
    a.out[hi * DM + 64 * cj + lane] = __builtin_bit_cast(u16, ob);
  }
}

extern "C" void kernel_launch(void* const* d_in, const int* in_sizes, int n_in,
                              void* d_out, int out_size, void* d_ws, size_t ws_size,
                              hipStream_t stream) {
  Params p;
  p.x = (const u16*)d_in[0];
  p.kc = (const u16*)d_in[1];
  p.vc = (const u16*)d_in[2];
  p.wq = (const u16*)d_in[3];
  p.wo = (const u16*)d_in[4];
  p.w1 = (const u16*)d_in[6];
  p.b1 = (const u16*)d_in[7];
  p.w2 = (const u16*)d_in[8];
  p.b2 = (const u16*)d_in[9];
  p.out = (u16*)d_out;
  p.ws = (u64*)d_ws;
  (void)hipMemsetAsync(d_ws, 0, G_END * 8, stream);
  hipLaunchKernelGGL(fused_decode_kernel, dim3(NWG), dim3(NTHR), 0, stream, p);
}
```

```cpp
#include <hip/hip_runtime.h>
#include <hip/hip_bf16.h>
#include <cstdint>

typedef unsigned int u32;
typedef unsigned long long u64;
typedef unsigned short u16;
using u32x4 = __attribute__((ext_vector_type(4))) u32;
using f32x4 = __attribute__((ext_vector_type(4))) float;
using s16x8 = __attribute__((ext_vector_type(8))) short;
using bf16x2 = __attribute__((ext_vector_type(2))) __bf16;

constexpr int NB = 8, DM = 512, HQ = 32, HKV = 8, SK = 4096, DH = 128, FF = 1792;
constexpr int NWG = 256, NTHR = 512;
constexpr u32 TAG = 0x5A17C3E1u;
constexpr u32 SPIN_CAP = 1u << 20;
constexpr float LOG2E = 1.4426950408889634f;

constexpr size_t G_Q = 0;
constexpr size_t G_H = G_Q + (size_t)NB * 2048;
constexpr size_t G_P = G_H + (size_t)NB * 896;
constexpr size_t G_S = G_P + (size_t)NB * HQ * 4 * 132;
constexpr size_t G_Y = G_S + (size_t)8 * NB * 32 * 64;
constexpr size_t G_END = G_Y + (size_t)8 * NB * 32 * 64;

__device__ __forceinline__ float bf16r(float x) { return (float)(__bf16)x; }
__device__ __forceinline__ u32 pack2(float lo, float hi) {
  bf16x2 v = {(__bf16)lo, (__bf16)hi};
  return __builtin_bit_cast(u32, v);
}
__device__ __forceinline__ float bflo(u32 x) { return __builtin_bit_cast(float, x << 16); }
__device__ __forceinline__ float bfhi(u32 x) { return __builtin_bit_cast(float, x & 0xffff0000u); }
__device__ __forceinline__ float bfu(u16 x) { return __builtin_bit_cast(float, (u32)x << 16); }
__device__ __forceinline__ float dot2(u32 k, u32 q, float acc) {
  return __builtin_amdgcn_fdot2_f32_bf16(__builtin_bit_cast(bf16x2, k), __builtin_bit_cast(bf16x2, q), acc, false);
}
template <int CTRL> __device__ __forceinline__ float dpp(float x) {
  return __builtin_bit_cast(float, __builtin_amdgcn_mov_dpp(__builtin_bit_cast(int, x), CTRL, 0xf, 0xf, true));
}
constexpr int XOR1 = 0xB1, XOR2 = 0x4E, XOR7 = 0x141, XOR8 = 0x128, ROR4 = 0x124;
__device__ __forceinline__ float xrow16_max(float x) {
  auto s = __builtin_amdgcn_permlane16_swap(__float_as_uint(x), __float_as_uint(x), false, false);
  x = fmaxf(__uint_as_float(s[0]), __uint_as_float(s[1]));
  auto t = __builtin_amdgcn_permlane32_swap(__float_as_uint(x), __float_as_uint(x), false, false);
  return fmaxf(__uint_as_float(t[0]), __uint_as_float(t[1]));
}
__device__ __forceinline__ float xrow16_sum(float x) {
  auto s = __builtin_amdgcn_permlane16_swap(__float_as_uint(x), __float_as_uint(x), false, false);
  x = __uint_as_float(s[0]) + __uint_as_float(s[1]);
  auto t = __builtin_amdgcn_permlane32_swap(__float_as_uint(x), __float_as_uint(x), false, false);
  return __uint_as_float(t[0]) + __uint_as_float(t[1]);
}
__device__ __forceinline__ void put_granule(u64* g, u32 value) {
  __hip_atomic_store(g, ((u64)TAG << 32) | (u64)value, __ATOMIC_RELAXED, __HIP_MEMORY_SCOPE_AGENT);
}
__device__ __forceinline__ u64 get_granule(const u64* g) {
  return __hip_atomic_load(g, __ATOMIC_RELAXED, __HIP_MEMORY_SCOPE_AGENT);
}
__device__ __forceinline__ bool tag_ok(u64 g) { return (u32)(g >> 32) == TAG; }
__device__ __forceinline__ void wg_barrier() {
  asm volatile("s_waitcnt lgkmcnt(0)" ::: "memory");
  __builtin_amdgcn_s_barrier();
  asm volatile("" ::: "memory");
}
__device__ __forceinline__ u32x4 ld_nt(const u16* p) {
  return __builtin_nontemporal_load(reinterpret_cast<const u32x4*>(p));
}
__device__ __forceinline__ u32 lds_flag_load(u32* f) {
  return (u32)__builtin_amdgcn_readfirstlane(__hip_atomic_load(f, __ATOMIC_RELAXED, __HIP_MEMORY_SCOPE_WORKGROUP));
}
__device__ __forceinline__ void lds_wait_ge(u32* f, u32 want) {
  for (u32 spins = 0; spins < SPIN_CAP * 8u; ++spins) {
    if (lds_flag_load(f) >= want) break;
    __builtin_amdgcn_s_sleep(1);
  }
  asm volatile("" ::: "memory");
}
__device__ __forceinline__ void probe_wait(const u64* g, bool active) {
  for (u32 spins = 0; spins < SPIN_CAP; ++spins) {
    bool ok = true;
    if (active) ok = tag_ok(get_granule(g));
    if (__all(ok)) break;
    __builtin_amdgcn_s_sleep(2);
  }
}

#define STAMP(k) do { } while (0)

struct Params {
  const u16* x; const u16* kc; const u16* vc; const u16* wq; const u16* wo;
  const u16* w1; const u16* b1; const u16* w2; const u16* b2;
  u16* out; u64* ws;
};

constexpr int WROW = 144;
constexpr int L_WO = 0;
constexpr int L_W2 = L_WO + 128 * WROW;
constexpr int L_SVC = L_W2 + 64 * WROW;
constexpr int L_Q = L_SVC + 4 * 2 * 8 * 16 * 4;
constexpr int L_H = L_Q + 1024;
constexpr int L_O = L_H + 16 * 32 * 4;
constexpr int L_G = L_O + 16 * 64 * 4;
constexpr int L_FLAG = L_G + 256;
constexpr int L_P = L_FLAG + 32;
constexpr int L_MRG = L_P + 8 * 144 * 4 * 4;
constexpr int L_WQS = L_MRG + 8 * 4 * 132 * 4;
constexpr int L_W1S = L_WQS + 4 * 4352;
constexpr int L_XN = L_W1S + 4 * 2304;
constexpr int L_END = L_XN + 64;

template <int ROLE>
__device__ __forceinline__ void body(const Params& a, unsigned char* lds, const int v) {
  constexpr int NG = (ROLE == 0) ? 1 : 9;
  const int w = blockIdx.x;
  const int tid = threadIdx.x, lane = tid & 63;
  const int r = lane >> 4, c = lane & 15;
  const int bh = w >> 2, split = w & 3;
  const int ab = bh >> 3, ahkv = bh & 7;
  const int hi = w >> 3, cj = w & 7;
  const int qb = (w & 7) * 32 + (w >> 3);
  const bool h_on = (qb < 224);
  u64* ws = a.ws;
  u32* flags = reinterpret_cast<u32*>(lds + L_FLAG);
  STAMP(16 + v);

  if (ROLE == 0) {
    if (lane < 8) flags[lane] = 0u;
  }
  wg_barrier();

  const int key0 = (ROLE == 0) ? 0 : 16 + 144 * (v - 1);
  const size_t kvbase = ((size_t)bh * SK + split * 1024 + key0) * DH + (size_t)r * DH + 8 * c;
  u32x4 kv[4 * NG];
  u32 b1v = 0;

  if (ROLE <= 1) {
    u32x4 xs[2], xa[4];
#pragma unroll
    for (int i = 0; i < 2; ++i) xs[i] = ld_nt(a.x + (2 * v + i) * DM + 8 * lane);
#pragma unroll
    for (int s = 0; s < 4; ++s) xa[s] = ld_nt(a.x + (c & 7) * DM + 128 * v + 32 * s + 8 * r);
    u32x4 wqr[4], w1r[2];
#pragma unroll
    for (int u = 0; u < 4; ++u)
      wqr[u] = ld_nt(a.wq + (size_t)(128 * v + 32 * u + (lane >> 1)) * 4096 + 16 * qb + 8 * (lane & 1));
#pragma unroll
    for (int u = 0; u < 2; ++u)
      w1r[u] = ld_nt(a.w1 + (size_t)(128 * v + 64 * u + lane) * FF + 8 * (h_on ? qb : 0));
    if (ROLE == 0) b1v = *reinterpret_cast<const u32*>(a.b1 + 8 * (h_on ? qb : 0) + 2 * (lane & 3));
    __builtin_amdgcn_sched_barrier(0);
    if (lane == 0) __hip_atomic_fetch_add(flags + 3, 1u, __ATOMIC_RELAXED, __HIP_MEMORY_SCOPE_WORKGROUP);
    float* stat = reinterpret_cast<float*>(lds + L_XN);
#pragma unroll
    for (int i = 0; i < 2; ++i) {
      float s = 0.f;
#pragma unroll
      for (int k = 0; k < 4; ++k) s += bflo(xs[i][k]) + bfhi(xs[i][k]);
      s += dpp<XOR1>(s); s += dpp<XOR2>(s); s += dpp<XOR7>(s); s += dpp<XOR8>(s); s = xrow16_sum(s);
      const float mean = s * (1.0f / DM);
      float q2 = 0.f;
#pragma unroll
      for (int k = 0; k < 4; ++k) {
        const float d0 = bflo(xs[i][k]) - mean, d1 = bfhi(xs[i][k]) - mean;
        q2 = fmaf(d0, d0, q2); q2 = fmaf(d1, d1, q2);
      }
      q2 += dpp<XOR1>(q2); q2 += dpp<XOR2>(q2); q2 += dpp<XOR7>(q2); q2 += dpp<XOR8>(q2); q2 = xrow16_sum(q2);
      const float var_bf = bf16r(q2 * (1.0f / DM));
      if (lane == 0) { stat[4 * v + 2 * i] = bf16r(mean); stat[4 * v + 2 * i + 1] = bf16r(sqrtf(bf16r(var_bf + 1e-5f))); }
    }
    asm volatile("s_waitcnt lgkmcnt(0)" ::: "memory");
    if (lane == 0) __hip_atomic_fetch_add(flags + 4, 1u, __ATOMIC_RELAXED, __HIP_MEMORY_SCOPE_WORKGROUP);
    if (v == 1) STAMP(12);
    unsigned char* wqs = lds + L_WQS + v * 4352;
    unsigned char* w1s = lds + L_W1S + v * 2304;
#pragma unroll
    for (int u = 0; u < 4; ++u) {
      const int rr = 32 * u + (lane >> 1);
      *reinterpret_cast<u32x4*>(wqs + rr * 32 + (rr >> 3) * 16 + 16 * (lane & 1)) = wqr[u];
    }
#pragma unroll
    for (int u = 0; u < 2; ++u) {
      const int rr = 64 * u + lane;
      *reinterpret_cast<u32x4*>(w1s + rr * 16 + (rr >> 3) * 16) = w1r[u];
    }
    lds_wait_ge(flags + 4, 4u);
    const float mean_bf = stat[2 * (c & 7)], sd_bf = stat[2 * (c & 7) + 1];
    asm volatile("s_waitcnt lgkmcnt(0)" ::: "memory");
    f32x4 accq = {0.f, 0.f, 0.f, 0.f}, acch = {0.f, 0.f, 0.f, 0.f};
#pragma unroll
    for (int s = 0; s < 4; ++s) {
      u32x4 af;
#pragma unroll
      for (int k = 0; k < 4; ++k)
        af[k] = (c < 8) ? pack2(bf16r(bflo(xa[s][k]) - mean_bf) / sd_bf, bf16r(bfhi(xa[s][k]) - mean_bf) / sd_bf) : 0u;
      const s16x8 afr = __builtin_bit_cast(s16x8, af);
      s16x8 bq, bws;
#pragma unroll
      for (int jj = 0; jj < 8; ++jj) {
        const int k = 32 * s + 8 * r + jj;
        bq[jj] = *reinterpret_cast<const short*>(wqs + k * 32 + (k >> 3) * 16 + c * 2);
        bws[jj] = *reinterpret_cast<const short*>(w1s + k * 16 + (k >> 3) * 16 + (c & 7) * 2);
      }
      if (c >= 8) bws = s16x8{0, 0, 0, 0, 0, 0, 0, 0};
      accq = __builtin_amdgcn_mfma_f32_16x16x32_bf16(afr, bq, accq, 0, 0, 0);
      acch = __builtin_amdgcn_mfma_f32_16x16x32_bf16(afr, bws, acch, 0, 0, 0);
    }
    if (v == 1) STAMP(13);
    float* svc = reinterpret_cast<float*>(lds + L_SVC) + v * 256;
    if (lane < 32) {
#pragma unroll
      for (int k = 0; k < 4; ++k) {
        svc[(4 * r + k) * 16 + c] = accq[k];
        svc[128 + (4 * r + k) * 16 + c] = acch[k];
      }
    }
    if (ROLE == 1) {
      asm volatile("s_waitcnt lgkmcnt(0)" ::: "memory");
      if (lane == 0) __hip_atomic_fetch_add(flags + 1, 1u, __ATOMIC_RELAXED, __HIP_MEMORY_SCOPE_WORKGROUP);
    }
  }
  STAMP(24 + v);

  if (ROLE == 2) {
    const int t4 = tid - 256;
    lds_wait_ge(flags + 3, 4u);
    u32x4 st[6];
#pragma unroll
    for (int u = 0; u < 4; ++u) {
      const int e = t4 + 256 * u, row = e >> 3, seg = e & 7;
      st[u] = ld_nt(a.wo + (size_t)(128 * hi + row) * DM + 64 * cj + 8 * seg);
    }
#pragma unroll
    for (int u = 0; u < 2; ++u) {
      const int e = t4 + 256 * u, row = e >> 3, seg = e & 7;
      st[4 + u] = ld_nt(a.w2 + (size_t)(56 * hi + (row < 56 ? row : 0)) * DM + 64 * cj + 8 * seg);
    }
#pragma unroll
    for (int i = 0; i < 4 * NG; ++i) kv[i] = ld_nt(a.kc + kvbase + (size_t)i * 4 * DH);
    __builtin_amdgcn_sched_barrier(0);
#pragma unroll
    for (int u = 0; u < 4; ++u) {
      const int e = t4 + 256 * u, row = e >> 3, seg = e & 7;
      *reinterpret_cast<u32x4*>(lds + L_WO + row * WROW + 16 * seg) = st[u];
    }
#pragma unroll
    for (int u = 0; u < 2; ++u) {
      const int e = t4 + 256 * u, row = e >> 3, seg = e & 7;
      *reinterpret_cast<u32x4*>(lds + L_W2 + row * WROW + 16 * seg) = row < 56 ? st[4 + u] : u32x4{0u, 0u, 0u, 0u};
    }
    reinterpret_cast<u32*>(lds + L_H)[256 + t4] = 0u;
    reinterpret_cast<u32*>(lds + L_O)[512 + t4] = 0u;
    reinterpret_cast<u32*>(lds + L_O)[768 + t4] = 0u;
    asm volatile("s_waitcnt lgkmcnt(0)" ::: "memory");
    if (lane == 0) __hip_atomic_fetch_add(flags + 2, 1u, __ATOMIC_RELAXED, __HIP_MEMORY_SCOPE_WORKGROUP);
  }
  if (ROLE == 1) {
#pragma unroll
    for (int i = 0; i < 4 * NG; ++i) kv[i] = ld_nt(a.kc + kvbase + (size_t)i * 4 * DH);
    __builtin_amdgcn_sched_barrier(0);
  }

  if (ROLE == 0) {
    lds_wait_ge(flags + 1, 3u);
    const float* svc = reinterpret_cast<const float*>(lds + L_SVC);
    {
      const int b = lane >> 3, cp = lane & 7;
      float s0 = 0.f, s1 = 0.f;
#pragma unroll
      for (int u = 0; u < 4; ++u) { s0 += svc[u * 256 + b * 16 + 2 * cp]; s1 += svc[u * 256 + b * 16 + 2 * cp + 1]; }
      put_granule(ws + G_Q + (size_t)b * 2048 + 8 * qb + cp, pack2(s0, s1));
    }
    if (h_on && lane < 32) {
      const int b = lane >> 2, cp = lane & 3;
      float hv[2];
#pragma unroll
      for (int e = 0; e < 2; ++e) {
        float s0 = 0.f;
#pragma unroll
        for (int u = 0; u < 4; ++u) s0 += svc[u * 256 + 128 + b * 16 + 2 * cp + e];
        const float t = bf16r(bf16r(s0) + (e ? bfhi(b1v) : bflo(b1v)));
        const float inner = 0.7978845608028654f * (t + 0.044715f * t * t * t);
        hv[e] = 0.5f * t * (1.0f + tanhf(inner));
      }
      put_granule(ws + G_H + (size_t)b * 896 + 4 * qb + cp, pack2(hv[0], hv[1]));
    }
    STAMP(1);
    const u64* qg = ws + G_Q + (size_t)ab * 2048 + ahkv * 256 + lane;
    u32 val[4], hval[4];
    const u64* hp[4];
    int hidx[4];
#pragma unroll
    for (int k = 0; k < 4; ++k) {
      const int idx = min(lane + 64 * k, 223);
      const int b = idx / 28, t = idx % 28;
      hp[k] = ws + G_H + (size_t)b * 896 + 28 * hi + t;
      hidx[k] = b * 32 + t;
    }
    for (u32 spins = 0; spins < SPIN_CAP; ++spins) {
      bool ok = true;
#pragma unroll
      for (int k = 0; k < 4; ++k) { const u64 g = get_granule(qg + 64 * k); val[k] = (u32)g; ok &= tag_ok(g); }
      if (__all(ok)) break;
      __builtin_amdgcn_s_sleep(1);
    }
    u32* qs = reinterpret_cast<u32*>(lds + L_Q);
#pragma unroll
    for (int k = 0; k < 4; ++k) qs[64 * k + lane] = val[k];
    asm volatile("s_waitcnt lgkmcnt(0)" ::: "memory");
    if (lane == 0) __hip_atomic_store(flags, 1u, __ATOMIC_RELAXED, __HIP_MEMORY_SCOPE_WORKGROUP);
    STAMP(2);
#pragma unroll
    for (int i = 0; i < 4 * NG; ++i) kv[i] = ld_nt(a.kc + kvbase + (size_t)i * 4 * DH);
    for (u32 spins = 0; spins < SPIN_CAP; ++spins) {
      bool ok = true;
#pragma unroll
      for (int k = 0; k < 4; ++k) { const u64 g = get_granule(hp[k]); hval[k] = (u32)g; ok &= tag_ok(g); }
      if (__all(ok)) break;
      __builtin_amdgcn_s_sleep(1);
    }
    u32* hs = reinterpret_cast<u32*>(lds + L_H);
#pragma unroll
    for (int k = 0; k < 4; ++k) if (lane + 64 * k < 224) hs[hidx[k]] = hval[k];
    if (lane < 32) hs[(lane >> 2) * 32 + 28 + (lane & 3)] = 0u;
    lds_wait_ge(flags + 2, 4u);
    asm volatile("s_waitcnt lgkmcnt(0)" ::: "memory");
#pragma unroll
    for (int ct = 0; ct < 4; ++ct) {
      f32x4 acc = {0.f, 0.f, 0.f, 0.f};
#pragma unroll
      for (int s = 0; s < 2; ++s) {
        const u32x4 af = *reinterpret_cast<const u32x4*>(lds + L_H + c * 128 + (32 * s + 8 * r) * 2);
        s16x8 bf;
#pragma unroll
        for (int jj = 0; jj < 8; ++jj)
          bf[jj] = *reinterpret_cast<const short*>(lds + L_W2 + (32 * s + 8 * r + jj) * WROW + (16 * ct + c) * 2);
        acc = __builtin_amdgcn_mfma_f32_16x16x32_bf16(__builtin_bit_cast(s16x8, af), bf, acc, 0, 0, 0);
      }
      if (lane < 32) {
#pragma unroll
        for (int k = 0; k < 4; ++k)
          put_granule(ws + G_Y + (((size_t)cj * NB + (4 * r + k)) * 32 + hi) * 64 + 16 * ct + c, __float_as_uint(acc[k]));
      }
    }
    STAMP(3);
  } else {
    lds_wait_ge(flags, 1u);
  }

  u32x4 qpk[4];
#pragma unroll
  for (int g = 0; g < 4; ++g) qpk[g] = *reinterpret_cast<const u32x4*>(lds + L_Q + g * 256 + c * 16);
  float sc[NG];
#pragma unroll
  for (int grp = 0; grp < NG; ++grp) {
    float x[16];
#pragma unroll
    for (int t = 0; t < 4; ++t)
#pragma unroll
      for (int g = 0; g < 4; ++g) {
        float d = 0.f;
#pragma unroll
        for (int k = 0; k < 4; ++k) d = dot2(kv[4 * grp + t][k], qpk[g][k], d);
        x[t * 4 + g] = d;
      }
#define TR_STEP(HALF, CTRL, BIT) _Pragma("unroll") for (int h = 0; h < (HALF); ++h) { \
      const float lo_ = x[h], hi_ = x[h + (HALF)]; const float t1_ = lo_ + dpp<CTRL>(lo_), t2_ = hi_ + dpp<CTRL>(hi_); \
      x[h] = (c & (BIT)) ? t2_ : t1_; }
    TR_STEP(8, XOR8, 8) TR_STEP(4, XOR7, 4) TR_STEP(2, XOR2, 2) TR_STEP(1, XOR1, 1)
#undef TR_STEP
    sc[grp] = bf16r(bf16r(x[0]) * 0.08838834764831845f);
#pragma unroll
    for (int t = 0; t < 4; ++t) kv[4 * grp + t] = ld_nt(a.vc + kvbase + (size_t)(4 * grp + t) * 4 * DH);
    __builtin_amdgcn_sched_barrier(0);
  }
  STAMP(48 + v);
  float m = sc[0];
#pragma unroll
  for (int grp = 1; grp < NG; ++grp) m = fmaxf(m, sc[grp]);
  m = fmaxf(m, dpp<ROR4>(m)); m = fmaxf(m, dpp<XOR8>(m)); m = xrow16_max(m);
  float l = 0.f;
  {
    float* pl = reinterpret_cast<float*>(lds + L_P) + v * 576;
#pragma unroll
    for (int grp = 0; grp < NG; ++grp) {
      const float p = __builtin_amdgcn_exp2f((sc[grp] - m) * LOG2E);
      l += p;
      pl[(16 * grp + 4 * (c >> 2) + r) * 4 + (c & 3)] = p;
    }
  }
  l += dpp<ROR4>(l); l += dpp<XOR8>(l); l = xrow16_sum(l);
  asm volatile("s_waitcnt lgkmcnt(0)" ::: "memory");

  float acc[32];
#pragma unroll
  for (int k = 0; k < 32; ++k) acc[k] = 0.f;
  {
    const f32x4* pl4 = reinterpret_cast<const f32x4*>(lds + L_P) + v * 144;
#pragma unroll
    for (int i = 0; i < 4 * NG; ++i) {
      const f32x4 p = pl4[4 * i + r];
      float vf[8];
#pragma unroll
      for (int k = 0; k < 4; ++k) { vf[2 * k] = bflo(kv[i][k]); vf[2 * k + 1] = bfhi(kv[i][k]); }
#pragma unroll
      for (int g = 0; g < 4; ++g)
#pragma unroll
        for (int k = 0; k < 8; ++k) acc[g * 8 + k] = fmaf(p[g], vf[k], acc[g * 8 + k]);
    }
  }
#pragma unroll
  for (int k = 0; k < 32; ++k) acc[k] = xrow16_sum(acc[k]);
  STAMP(40 + v);
  {
    float* mrg = reinterpret_cast<float*>(lds + L_MRG) + v * 4 * 132;
    if (r == 0) {
#pragma unroll
      for (int g = 0; g < 4; ++g) {
        *reinterpret_cast<f32x4*>(mrg + g * 132 + 4 + 8 * c) = f32x4{acc[g * 8], acc[g * 8 + 1], acc[g * 8 + 2], acc[g * 8 + 3]};
        *reinterpret_cast<f32x4*>(mrg + g * 132 + 4 + 8 * c + 4) = f32x4{acc[g * 8 + 4], acc[g * 8 + 5], acc[g * 8 + 6], acc[g * 8 + 7]};
      }
      if (c < 4) { mrg[c * 132] = m; mrg[c * 132 + 1] = l; }
    }
  }
  if (ROLE == 0 && hi < 8) {
    const u64* yg = ws + G_Y + ((size_t)cj * NB + hi) * 32 * 64 + lane;
    float ysum = 0.f;
    for (u32 spins = 0; spins < SPIN_CAP; ++spins) {
      bool ok = true; ysum = 0.f;
#pragma unroll
      for (int i = 0; i < 32; ++i) { const u64 g = get_granule(yg + 64 * i); ysum += __uint_as_float((u32)g); ok &= tag_ok(g); }
      if (__all(ok)) break;
      __builtin_amdgcn_s_sleep(8);
    }
    const float y = bf16r(bf16r(ysum) + bfu(a.b2[64 * cj + lane]));
    const float gate = bf16r(y / (1.0f + __expf(-y)));
    reinterpret_cast<float*>(lds + L_G)[lane] = gate;
  }
  wg_barrier();
  if (v == 0) STAMP(7);

  {
    const int g = tid >> 7, d = tid & 127;
    const float* mrg = reinterpret_cast<const float*>(lds + L_MRG) + g * 132;
    float M = mrg[0];
#pragma unroll
    for (int u = 1; u < 8; ++u) M = fmaxf(M, mrg[u * 4 * 132]);
    float L = 0.f, o = 0.f;
#pragma unroll
    for (int u = 0; u < 8; ++u) {
      const float wt = __builtin_amdgcn_exp2f((mrg[u * 4 * 132] - M) * LOG2E);
      L = fmaf(wt, mrg[u * 4 * 132 + 1], L);
      o = fmaf(wt, mrg[u * 4 * 132 + 4 + d], o);
    }
    u64* pg = ws + G_P + (((size_t)ab * HQ + ahkv * 4 + g) * 4 + split) * 132;
    put_granule(pg + d, __float_as_uint(o));
    if (d == 0) { put_granule(pg + 128, __float_as_uint(M)); put_granule(pg + 129, __float_as_uint(L)); }
  }

  {
    const u64* pg = ws + G_P + ((size_t)v * HQ + hi) * 4 * 132;
    probe_wait(pg + (lane & 3) * 132 + 129, lane < 4);
    u32 o0[4], o1[4], mm[4], ll[4];
    for (u32 spins = 0; spins < SPIN_CAP; ++spins) {
      bool ok = true;
#pragma unroll
      for (int s = 0; s < 4; ++s) {
        const u64 g0 = get_granule(pg + s * 132 + 2 * lane), g1 = get_granule(pg + s * 132 + 2 * lane + 1);
        const u64 gm = get_granule(pg + s * 132 + 128), gl = get_granule(pg + s * 132 + 129);
        o0[s] = (u32)g0; o1[s] = (u32)g1; mm[s] = (u32)gm; ll[s] = (u32)gl;
        ok &= tag_ok(g0) & tag_ok(g1) & tag_ok(gm) & tag_ok(gl);
      }
      if (__all(ok)) break;
      __builtin_amdgcn_s_sleep(2);
    }
    float M = __uint_as_float(mm[0]);
#pragma unroll
    for (int s = 1; s < 4; ++s) M = fmaxf(M, __uint_as_float(mm[s]));
    float L = 0.f, a0 = 0.f, a1 = 0.f;
#pragma unroll
    for (int s = 0; s < 4; ++s) {
      const float wt = __builtin_amdgcn_exp2f((__uint_as_float(mm[s]) - M) * LOG2E);
      L = fmaf(wt, __uint_as_float(ll[s]), L);
      a0 = fmaf(wt, __uint_as_float(o0[s]), a0);
      a1 = fmaf(wt, __uint_as_float(o1[s]), a1);
    }
    const float inv = 1.0f / L;
    reinterpret_cast<u32*>(lds + L_O)[v * 64 + lane] = pack2(a0 * inv, a1 * inv);
  }
  wg_barrier();
  if (v == 0) STAMP(8);
  if (ROLE <= 1) {
    const int ct = v;
    f32x4 acc4 = {0.f, 0.f, 0.f, 0.f};
#pragma unroll
    for (int s = 0; s < 4; ++s) {
      const u32x4 af = *reinterpret_cast<const u32x4*>(lds + L_O + c * 256 + (32 * s + 8 * r) * 2);
      s16x8 bf;
#pragma unroll
      for (int jj = 0; jj < 8; ++jj)
        bf[jj] = *reinterpret_cast<const short*>(lds + L_WO + (32 * s + 8 * r + jj) * WROW + (16 * ct + c) * 2);
      acc4 = __builtin_amdgcn_mfma_f32_16x16x32_bf16(__builtin_bit_cast(s16x8, af), bf, acc4, 0, 0, 0);
    }
    if (lane < 32) {
#pragma unroll
      for (int k = 0; k < 4; ++k)
        put_granule(ws + G_S + (((size_t)cj * NB + (4 * r + k)) * 32 + hi) * 64 + 16 * ct + c, __float_as_uint(acc4[k]));
    }
  }
  if (ROLE == 2 && hi < 8 && v == 4) {
    const u64* sg = ws + G_S + ((size_t)cj * NB + hi) * 32 * 64;
    probe_wait(sg + (lane >> 1) * 64 + 63 - (lane & 1) * 32, true);
    float ssum = 0.f;
    for (u32 spins = 0; spins < SPIN_CAP; ++spins) {
      bool ok = true; ssum = 0.f;
#pragma unroll
      for (int i = 0; i < 32; ++i) { const u64 g = get_granule(sg + 64 * i + lane); ssum += __uint_as_float((u32)g); ok &= tag_ok(g); }
      if (__all(ok)) break;
      __builtin_amdgcn_s_sleep(2);
    }
    const float sb = bf16r(ssum);
    const float gate = reinterpret_cast<const float*>(lds + L_G)[lane];
    const float xo = bfu(a.x[hi * DM + 64 * cj + lane]);
    const float res = xo + bf16r(sb * gate);
    const __bf16 ob = (__bf16)res;
    a.out[hi * DM + 64 * cj + lane] = __builtin_bit_cast(u16, ob);
    STAMP(9);
  }
}

__global__ void __launch_bounds__(NTHR, 2) fused_decode_kernel(Params a) {
  __shared__ __attribute__((aligned(16))) unsigned char lds[L_END];
  const int v = __builtin_amdgcn_readfirstlane(threadIdx.x >> 6);
  if (v == 0) body<0>(a, lds, 0);
  else if (v < 4) body<1>(a, lds, v);
  else body<2>(a, lds, v);
}

extern "C" void kernel_launch(void* const* d_in, const int* in_sizes, int n_in,
                              void* d_out, int out_size, void* d_ws, size_t ws_size,
                              hipStream_t stream) {
  Params p;
  p.x = (const u16*)d_in[0];
  p.kc = (const u16*)d_in[1];
  p.vc = (const u16*)d_in[2];
  p.wq = (const u16*)d_in[3];
  p.wo = (const u16*)d_in[4];
  p.w1 = (const u16*)d_in[6];
  p.b1 = (const u16*)d_in[7];
  p.w2 = (const u16*)d_in[8];
  p.b2 = (const u16*)d_in[9];
  p.out = (u16*)d_out;
  p.ws = (u64*)d_ws;
  (void)hipMemsetAsync(d_ws, 0, G_END * 8, stream);
  hipLaunchKernelGGL(fused_decode_kernel, dim3(NWG), dim3(NTHR), 0, stream, p);
}
```

```cpp
#include <hip/hip_runtime.h>
#include <hip/hip_bf16.h>
#include <cstdint>

typedef unsigned int u32;
typedef unsigned long long u64;
typedef unsigned short u16;
using u32x4 = __attribute__((ext_vector_type(4))) u32;
using f32x4 = __attribute__((ext_vector_type(4))) float;
using s16x8 = __attribute__((ext_vector_type(8))) short;
using bf16x2 = __attribute__((ext_vector_type(2))) __bf16;

constexpr int NB = 8, DM = 512, HQ = 32, HKV = 8, SK = 4096, DH = 128, FF = 1792;
constexpr int NWG = 256, NTHR = 512;
constexpr u32 TAG = 0x5A17C3E1u;
constexpr u32 SPIN_CAP = 1u << 20;
constexpr float LOG2E = 1.4426950408889634f;

constexpr size_t G_Q = 0;
constexpr size_t G_H = G_Q + (size_t)NB * 2048;
constexpr size_t G_P = G_H + (size_t)NB * 896;
constexpr size_t G_S = G_P + (size_t)NB * HQ * 4 * 132;
constexpr size_t G_Y = G_S + (size_t)8 * NB * 32 * 64;
constexpr size_t G_END = G_Y + (size_t)8 * NB * 32 * 64;

__device__ __forceinline__ float bf16r(float x) { return (float)(__bf16)x; }
__device__ __forceinline__ u32 pack2(float lo, float hi) {
  bf16x2 v = {(__bf16)lo, (__bf16)hi};
  return __builtin_bit_cast(u32, v);
}
__device__ __forceinline__ float bflo(u32 x) { return __builtin_bit_cast(float, x << 16); }
__device__ __forceinline__ float bfhi(u32 x) { return __builtin_bit_cast(float, x & 0xffff0000u); }
__device__ __forceinline__ float bfu(u16 x) { return __builtin_bit_cast(float, (u32)x << 16); }
__device__ __forceinline__ float dot2(u32 k, u32 q, float acc) {
  return __builtin_amdgcn_fdot2_f32_bf16(__builtin_bit_cast(bf16x2, k), __builtin_bit_cast(bf16x2, q), acc, false);
}
template <int CTRL> __device__ __forceinline__ float dpp(float x) {
  return __builtin_bit_cast(float, __builtin_amdgcn_mov_dpp(__builtin_bit_cast(int, x), CTRL, 0xf, 0xf, true));
}
constexpr int XOR1 = 0xB1, XOR2 = 0x4E, XOR7 = 0x141, XOR8 = 0x128, ROR4 = 0x124;
__device__ __forceinline__ float xrow16_max(float x) {
  auto s = __builtin_amdgcn_permlane16_swap(__float_as_uint(x), __float_as_uint(x), false, false);
  x = fmaxf(__uint_as_float(s[0]), __uint_as_float(s[1]));
  auto t = __builtin_amdgcn_permlane32_swap(__float_as_uint(x), __float_as_uint(x), false, false);
  return fmaxf(__uint_as_float(t[0]), __uint_as_float(t[1]));
}
__device__ __forceinline__ float xrow16_sum(float x) {
  auto s = __builtin_amdgcn_permlane16_swap(__float_as_uint(x), __float_as_uint(x), false, false);
  x = __uint_as_float(s[0]) + __uint_as_float(s[1]);
  auto t = __builtin_amdgcn_permlane32_swap(__float_as_uint(x), __float_as_uint(x), false, false);
  return __uint_as_float(t[0]) + __uint_as_float(t[1]);
}
__device__ __forceinline__ void put_granule(u64* g, u32 value) {
  __hip_atomic_store(g, ((u64)TAG << 32) | (u64)value, __ATOMIC_RELAXED, __HIP_MEMORY_SCOPE_AGENT);
}
__device__ __forceinline__ u64 get_granule(const u64* g) {
  return __hip_atomic_load(g, __ATOMIC_RELAXED, __HIP_MEMORY_SCOPE_AGENT);
}
__device__ __forceinline__ bool tag_ok(u64 g) { return (u32)(g >> 32) == TAG; }
__device__ __forceinline__ void wg_barrier() {
  asm volatile("s_waitcnt lgkmcnt(0)" ::: "memory");
  __builtin_amdgcn_s_barrier();
  asm volatile("" ::: "memory");
}
__device__ __forceinline__ u32x4 ld_nt(const u16* p) {
  return __builtin_nontemporal_load(reinterpret_cast<const u32x4*>(p));
}
__device__ __forceinline__ u32 lds_flag_load(u32* f) {
  return (u32)__builtin_amdgcn_readfirstlane(__hip_atomic_load(f, __ATOMIC_RELAXED, __HIP_MEMORY_SCOPE_WORKGROUP));
}
__device__ __forceinline__ void lds_wait_ge(u32* f, u32 want) {
  for (u32 spins = 0; spins < SPIN_CAP * 8u; ++spins) {
    if (lds_flag_load(f) >= want) break;
    __builtin_amdgcn_s_sleep(1);
  }
  asm volatile("" ::: "memory");
}
__device__ __forceinline__ void probe_wait(const u64* g, bool active) {
  for (u32 spins = 0; spins < SPIN_CAP; ++spins) {
    bool ok = true;
    if (active) ok = tag_ok(get_granule(g));
    if (__all(ok)) break;
    __builtin_amdgcn_s_sleep(2);
  }
}

#define STAMP(k) do { } while (0)

struct Params {
  const u16* x; const u16* kc; const u16* vc; const u16* wq; const u16* wo;
  const u16* w1; const u16* b1; const u16* w2; const u16* b2;
  u16* out; u64* ws;
};

constexpr int WROW = 144;
constexpr int L_WO = 0;
constexpr int L_W2 = L_WO + 128 * WROW;
constexpr int L_SVC = L_W2 + 64 * WROW;
constexpr int L_Q = L_SVC + 4 * 2 * 8 * 16 * 4;
constexpr int L_H = L_Q + 1024;
constexpr int L_O = L_H + 16 * 32 * 4;
constexpr int L_G = L_O + 16 * 64 * 4;
constexpr int L_FLAG = L_G + 256;
constexpr int L_P = L_FLAG + 32;
constexpr int L_MRG = L_P + 8 * 144 * 4 * 4;
constexpr int L_WQS = L_MRG + 8 * 4 * 132 * 4;
constexpr int L_W1S = L_WQS + 4 * 4352;
constexpr int L_XN = L_W1S + 4 * 2304;
constexpr int L_END = L_XN + 64;

template <int ROLE>
__device__ __forceinline__ void body(const Params& a, unsigned char* lds, const int v) {
  constexpr int NG = (ROLE == 0) ? 1 : 9;
  const int w = blockIdx.x;
  const int tid = threadIdx.x, lane = tid & 63;
  const int r = lane >> 4, c = lane & 15;
  const int bh = w >> 2, split = w & 3;
  const int ab = bh >> 3, ahkv = bh & 7;
  const int hi = w >> 3, cj = w & 7;
  const int qb = (w & 7) * 32 + (w >> 3);
  const bool h_on = (qb < 224);
  u64* ws = a.ws;
  u32* flags = reinterpret_cast<u32*>(lds + L_FLAG);
  STAMP(16 + v);

  if (ROLE == 0) {
    if (lane < 8) flags[lane] = 0u;
  }
  wg_barrier();

  const int key0 = (ROLE == 0) ? 0 : 16 + 144 * (v - 1);
  const size_t kvbase = ((size_t)bh * SK + split * 1024 + key0) * DH + (size_t)r * DH + 8 * c;
  u32x4 kv[4 * NG];
  u32 b1v = 0;

  if (ROLE <= 1) {
    u32x4 xs[2], xa[4];
#pragma unroll
    for (int i = 0; i < 2; ++i) xs[i] = ld_nt(a.x + (2 * v + i) * DM + 8 * lane);
#pragma unroll
    for (int s = 0; s < 4; ++s) xa[s] = ld_nt(a.x + (c & 7) * DM + 128 * v + 32 * s + 8 * r);
    u32x4 wqr[4], w1r[2];
#pragma unroll
    for (int u = 0; u < 4; ++u)
      wqr[u] = ld_nt(a.wq + (size_t)(128 * v + 32 * u + (lane >> 1)) * 4096 + 16 * qb + 8 * (lane & 1));
#pragma unroll
    for (int u = 0; u < 2; ++u)
      w1r[u] = ld_nt(a.w1 + (size_t)(128 * v + 64 * u + lane) * FF + 8 * (h_on ? qb : 0));
    if (ROLE == 0) b1v = *reinterpret_cast<const u32*>(a.b1 + 8 * (h_on ? qb : 0) + 2 * (lane & 3));
    __builtin_amdgcn_sched_barrier(0);
    if (lane == 0) __hip_atomic_fetch_add(flags + 3, 1u, __ATOMIC_RELAXED, __HIP_MEMORY_SCOPE_WORKGROUP);
    float* stat = reinterpret_cast<float*>(lds + L_XN);
#pragma unroll
    for (int i = 0; i < 2; ++i) {
      float s = 0.f;
#pragma unroll
      for (int k = 0; k < 4; ++k) s += bflo(xs[i][k]) + bfhi(xs[i][k]);
      s += dpp<XOR1>(s); s += dpp<XOR2>(s); s += dpp<XOR7>(s); s += dpp<XOR8>(s); s = xrow16_sum(s);
      const float mean = s * (1.0f / DM);
      float q2 = 0.f;
#pragma unroll
      for (int k = 0; k < 4; ++k) {
        const float d0 = bflo(xs[i][k]) - mean, d1 = bfhi(xs[i][k]) - mean;
        q2 = fmaf(d0, d0, q2); q2 = fmaf(d1, d1, q2);
      }
      q2 += dpp<XOR1>(q2); q2 += dpp<XOR2>(q2); q2 += dpp<XOR7>(q2); q2 += dpp<XOR8>(q2); q2 = xrow16_sum(q2);
      const float var_bf = bf16r(q2 * (1.0f / DM));
      if (lane == 0) { stat[4 * v + 2 * i] = bf16r(mean); stat[4 * v + 2 * i + 1] = bf16r(sqrtf(bf16r(var_bf + 1e-5f))); }
    }
    asm volatile("s_waitcnt lgkmcnt(0)" ::: "memory");
    if (lane == 0) __hip_atomic_fetch_add(flags + 4, 1u, __ATOMIC_RELAXED, __HIP_MEMORY_SCOPE_WORKGROUP);
    if (v == 1) STAMP(12);
    unsigned char* wqs = lds + L_WQS + v * 4352;
    unsigned char* w1s = lds + L_W1S + v * 2304;
#pragma unroll
    for (int u = 0; u < 4; ++u) {
      const int rr = 32 * u + (lane >> 1);
      *reinterpret_cast<u32x4*>(wqs + rr * 32 + (rr >> 3) * 16 + 16 * (lane & 1)) = wqr[u];
    }
#pragma unroll
    for (int u = 0; u < 2; ++u) {
      const int rr = 64 * u + lane;
      *reinterpret_cast<u32x4*>(w1s + rr * 16 + (rr >> 3) * 16) = w1r[u];
    }
    lds_wait_ge(flags + 4, 4u);
    const float mean_bf = stat[2 * (c & 7)], sd_bf = stat[2 * (c & 7) + 1];
    asm volatile("s_waitcnt lgkmcnt(0)" ::: "memory");
    f32x4 accq = {0.f, 0.f, 0.f, 0.f}, acch = {0.f, 0.f, 0.f, 0.f};
#pragma unroll
    for (int s = 0; s < 4; ++s) {
      u32x4 af;
#pragma unroll
      for (int k = 0; k < 4; ++k)
        af[k] = (c < 8) ? pack2(bf16r(bflo(xa[s][k]) - mean_bf) / sd_bf, bf16r(bfhi(xa[s][k]) - mean_bf) / sd_bf) : 0u;
      const s16x8 afr = __builtin_bit_cast(s16x8, af);
      s16x8 bq, bws;
#pragma unroll
      for (int jj = 0; jj < 8; ++jj) {
        const int k = 32 * s + 8 * r + jj;
        bq[jj] = *reinterpret_cast<const short*>(wqs + k * 32 + (k >> 3) * 16 + c * 2);
        bws[jj] = *reinterpret_cast<const short*>(w1s + k * 16 + (k >> 3) * 16 + (c & 7) * 2);
      }
      if (c >= 8) bws = s16x8{0, 0, 0, 0, 0, 0, 0, 0};
      accq = __builtin_amdgcn_mfma_f32_16x16x32_bf16(afr, bq, accq, 0, 0, 0);
      acch = __builtin_amdgcn_mfma_f32_16x16x32_bf16(afr, bws, acch, 0, 0, 0);
    }
    if (v == 1) STAMP(13);
    float* svc = reinterpret_cast<float*>(lds + L_SVC) + v * 256;
    if (lane < 32) {
#pragma unroll
      for (int k = 0; k < 4; ++k) {
        svc[(4 * r + k) * 16 + c] = accq[k];
        svc[128 + (4 * r + k) * 16 + c] = acch[k];
      }
    }
    if (ROLE == 1) {
      asm volatile("s_waitcnt lgkmcnt(0)" ::: "memory");
      if (lane == 0) __hip_atomic_fetch_add(flags + 1, 1u, __ATOMIC_RELAXED, __HIP_MEMORY_SCOPE_WORKGROUP);
    }
  }
  STAMP(24 + v);

  if (ROLE == 2) {
    const int t4 = tid - 256;
    lds_wait_ge(flags + 3, 4u);
    u32x4 st[6];
#pragma unroll
    for (int u = 0; u < 4; ++u) {
      const int e = t4 + 256 * u, row = e >> 3, seg = e & 7;
      st[u] = ld_nt(a.wo + (size_t)(128 * hi + row) * DM + 64 * cj + 8 * seg);
    }
#pragma unroll
    for (int u = 0; u < 2; ++u) {
      const int e = t4 + 256 * u, row = e >> 3, seg = e & 7;
      st[4 + u] = ld_nt(a.w2 + (size_t)(56 * hi + (row < 56 ? row : 0)) * DM + 64 * cj + 8 * seg);
    }
#pragma unroll
    for (int i = 0; i < 4 * NG; ++i) kv[i] = ld_nt(a.kc + kvbase + (size_t)i * 4 * DH);
    __builtin_amdgcn_sched_barrier(0);
#pragma unroll
    for (int u = 0; u < 4; ++u) {
      const int e = t4 + 256 * u, row = e >> 3, seg = e & 7;
      *reinterpret_cast<u32x4*>(lds + L_WO + row * WROW + 16 * seg) = st[u];
    }
#pragma unroll
    for (int u = 0; u < 2; ++u) {
      const int e = t4 + 256 * u, row = e >> 3, seg = e & 7;
      *reinterpret_cast<u32x4*>(lds + L_W2 + row * WROW + 16 * seg) = row < 56 ? st[4 + u] : u32x4{0u, 0u, 0u, 0u};
    }
    reinterpret_cast<u32*>(lds + L_H)[256 + t4] = 0u;
    reinterpret_cast<u32*>(lds + L_O)[512 + t4] = 0u;
    reinterpret_cast<u32*>(lds + L_O)[768 + t4] = 0u;
    asm volatile("s_waitcnt lgkmcnt(0)" ::: "memory");
    if (lane == 0) __hip_atomic_fetch_add(flags + 2, 1u, __ATOMIC_RELAXED, __HIP_MEMORY_SCOPE_WORKGROUP);
  }
  if (ROLE == 1) {
#pragma unroll
    for (int i = 0; i < 4 * NG; ++i) kv[i] = ld_nt(a.kc + kvbase + (size_t)i * 4 * DH);
    __builtin_amdgcn_sched_barrier(0);
  }

  if (ROLE == 0) {
    lds_wait_ge(flags + 1, 3u);
    const float* svc = reinterpret_cast<const float*>(lds + L_SVC);
    {
      const int b = lane >> 3, cp = lane & 7;
      float s0 = 0.f, s1 = 0.f;
#pragma unroll
      for (int u = 0; u < 4; ++u) { s0 += svc[u * 256 + b * 16 + 2 * cp]; s1 += svc[u * 256 + b * 16 + 2 * cp + 1]; }
      put_granule(ws + G_Q + (size_t)b * 2048 + 8 * qb + cp, pack2(s0, s1));
    }
    if (h_on && lane < 32) {
      const int b = lane >> 2, cp = lane & 3;
      float hv[2];
#pragma unroll
      for (int e = 0; e < 2; ++e) {
        float s0 = 0.f;
#pragma unroll
        for (int u = 0; u < 4; ++u) s0 += svc[u * 256 + 128 + b * 16 + 2 * cp + e];
        const float t = bf16r(bf16r(s0) + (e ? bfhi(b1v) : bflo(b1v)));
        const float inner = 0.7978845608028654f * (t + 0.044715f * t * t * t);
        hv[e] = 0.5f * t * (1.0f + tanhf(inner));
      }
      put_granule(ws + G_H + (size_t)b * 896 + 4 * qb + cp, pack2(hv[0], hv[1]));
    }
    STAMP(1);
    const u64* qg = ws + G_Q + (size_t)ab * 2048 + ahkv * 256 + lane;
    u32 val[4], hval[4];
    const u64* hp[4];
    int hidx[4];
#pragma unroll
    for (int k = 0; k < 4; ++k) {
      const int idx = min(lane + 64 * k, 223);
      const int b = idx / 28, t = idx % 28;
      hp[k] = ws + G_H + (size_t)b * 896 + 28 * hi + t;
      hidx[k] = b * 32 + t;
    }
    for (u32 spins = 0; spins < SPIN_CAP; ++spins) {
      bool ok = true;
#pragma unroll
      for (int k = 0; k < 4; ++k) { const u64 g = get_granule(qg + 64 * k); val[k] = (u32)g; ok &= tag_ok(g); }
      if (__all(ok)) break;
      __builtin_amdgcn_s_sleep(1);
    }
    u32* qs = reinterpret_cast<u32*>(lds + L_Q);
#pragma unroll
    for (int k = 0; k < 4; ++k) qs[64 * k + lane] = val[k];
    asm volatile("s_waitcnt lgkmcnt(0)" ::: "memory");
    if (lane == 0) __hip_atomic_store(flags, 1u, __ATOMIC_RELAXED, __HIP_MEMORY_SCOPE_WORKGROUP);
    STAMP(2);
#pragma unroll
    for (int i = 0; i < 4 * NG; ++i) kv[i] = ld_nt(a.kc + kvbase + (size_t)i * 4 * DH);
    for (u32 spins = 0; spins < SPIN_CAP; ++spins) {
      bool ok = true;
#pragma unroll
      for (int k = 0; k < 4; ++k) { const u64 g = get_granule(hp[k]); hval[k] = (u32)g; ok &= tag_ok(g); }
      if (__all(ok)) break;
      __builtin_amdgcn_s_sleep(1);
    }
    u32* hs = reinterpret_cast<u32*>(lds + L_H);
#pragma unroll
    for (int k = 0; k < 4; ++k) if (lane + 64 * k < 224) hs[hidx[k]] = hval[k];
    if (lane < 32) hs[(lane >> 2) * 32 + 28 + (lane & 3)] = 0u;
    lds_wait_ge(flags + 2, 4u);
    asm volatile("s_waitcnt lgkmcnt(0)" ::: "memory");
#pragma unroll
    for (int ct = 0; ct < 4; ++ct) {
      f32x4 acc = {0.f, 0.f, 0.f, 0.f};
#pragma unroll
      for (int s = 0; s < 2; ++s) {
        const u32x4 af = *reinterpret_cast<const u32x4*>(lds + L_H + c * 128 + (32 * s + 8 * r) * 2);
        s16x8 bf;
#pragma unroll
        for (int jj = 0; jj < 8; ++jj)
          bf[jj] = *reinterpret_cast<const short*>(lds + L_W2 + (32 * s + 8 * r + jj) * WROW + (16 * ct + c) * 2);
        acc = __builtin_amdgcn_mfma_f32_16x16x32_bf16(__builtin_bit_cast(s16x8, af), bf, acc, 0, 0, 0);
      }
      if (lane < 32) {
#pragma unroll
        for (int k = 0; k < 4; ++k)
          put_granule(ws + G_Y + (((size_t)cj * NB + (4 * r + k)) * 32 + hi) * 64 + 16 * ct + c, __float_as_uint(acc[k]));
      }
    }
    STAMP(3);
  } else {
    lds_wait_ge(flags, 1u);
  }

  u32x4 qpk[4];
#pragma unroll
  for (int g = 0; g < 4; ++g) qpk[g] = *reinterpret_cast<const u32x4*>(lds + L_Q + g * 256 + c * 16);
  float sc[NG];
#pragma unroll
  for (int grp = 0; grp < NG; ++grp) {
    float x[16];
#pragma unroll
    for (int t = 0; t < 4; ++t)
#pragma unroll
      for (int g = 0; g < 4; ++g) {
        float d = 0.f;
#pragma unroll
        for (int k = 0; k < 4; ++k) d = dot2(kv[4 * grp + t][k], qpk[g][k], d);
        x[t * 4 + g] = d;
      }
#define TR_STEP(HALF, CTRL, BIT) _Pragma("unroll") for (int h = 0; h < (HALF); ++h) { \
      const float lo_ = x[h], hi_ = x[h + (HALF)]; const float t1_ = lo_ + dpp<CTRL>(lo_), t2_ = hi_ + dpp<CTRL>(hi_); \
      x[h] = (c & (BIT)) ? t2_ : t1_; }
    TR_STEP(8, XOR8, 8) TR_STEP(4, XOR7, 4) TR_STEP(2, XOR2, 2) TR_STEP(1, XOR1, 1)
#undef TR_STEP
    sc[grp] = bf16r(bf16r(x[0]) * 0.08838834764831845f);
#pragma unroll
    for (int t = 0; t < 4; ++t) kv[4 * grp + t] = ld_nt(a.vc + kvbase + (size_t)(4 * grp + t) * 4 * DH);
    __builtin_amdgcn_sched_barrier(0);
  }
  STAMP(48 + v);
  float m = sc[0];
#pragma unroll
  for (int grp = 1; grp < NG; ++grp) m = fmaxf(m, sc[grp]);
  m = fmaxf(m, dpp<ROR4>(m)); m = fmaxf(m, dpp<XOR8>(m)); m = xrow16_max(m);
  float l = 0.f;
  {
    float* pl = reinterpret_cast<float*>(lds + L_P) + v * 576;
#pragma unroll
    for (int grp = 0; grp < NG; ++grp) {
      const float p = __builtin_amdgcn_exp2f((sc[grp] - m) * LOG2E);
      l += p;
      pl[(16 * grp + 4 * (c >> 2) + r) * 4 + (c & 3)] = p;
    }
  }
  l += dpp<ROR4>(l); l += dpp<XOR8>(l); l = xrow16_sum(l);
  asm volatile("s_waitcnt lgkmcnt(0)" ::: "memory");

  float acc[32];
#pragma unroll
  for (int k = 0; k < 32; ++k) acc[k] = 0.f;
  {
    const f32x4* pl4 = reinterpret_cast<const f32x4*>(lds + L_P) + v * 144;
#pragma unroll
    for (int i = 0; i < 4 * NG; ++i) {
      const f32x4 p = pl4[4 * i + r];
      float vf[8];
#pragma unroll
      for (int k = 0; k < 4; ++k) { vf[2 * k] = bflo(kv[i][k]); vf[2 * k + 1] = bfhi(kv[i][k]); }
#pragma unroll
      for (int g = 0; g < 4; ++g)
#pragma unroll
        for (int k = 0; k < 8; ++k) acc[g * 8 + k] = fmaf(p[g], vf[k], acc[g * 8 + k]);
    }
  }
#pragma unroll
  for (int k = 0; k < 32; ++k) acc[k] = xrow16_sum(acc[k]);
  STAMP(40 + v);
  {
    float* mrg = reinterpret_cast<float*>(lds + L_MRG) + v * 4 * 132;
    if (r == 0) {
#pragma unroll
      for (int g = 0; g < 4; ++g) {
        *reinterpret_cast<f32x4*>(mrg + g * 132 + 4 + 8 * c) = f32x4{acc[g * 8], acc[g * 8 + 1], acc[g * 8 + 2], acc[g * 8 + 3]};
        *reinterpret_cast<f32x4*>(mrg + g * 132 + 4 + 8 * c + 4) = f32x4{acc[g * 8 + 4], acc[g * 8 + 5], acc[g * 8 + 6], acc[g * 8 + 7]};
      }
      if (c < 4) { mrg[c * 132] = m; mrg[c * 132 + 1] = l; }
    }
  }
  if (ROLE == 0 && hi < 8) {
    const u64* yg = ws + G_Y + ((size_t)cj * NB + hi) * 32 * 64 + lane;
    float ysum = 0.f;
    for (u32 spins = 0; spins < SPIN_CAP; ++spins) {
      bool ok = true; ysum = 0.f;
#pragma unroll
      for (int i = 0; i < 32; ++i) { const u64 g = get_granule(yg + 64 * i); ysum += __uint_as_float((u32)g); ok &= tag_ok(g); }
      if (__all(ok)) break;
      __builtin_amdgcn_s_sleep(8);
    }
    const float y = bf16r(bf16r(ysum) + bfu(a.b2[64 * cj + lane]));
    const float gate = bf16r(y / (1.0f + __expf(-y)));
    reinterpret_cast<float*>(lds + L_G)[lane] = gate;
  }
  wg_barrier();
  if (v == 0) STAMP(7);

  {
    const int g = tid >> 7, d = tid & 127;
    const float* mrg = reinterpret_cast<const float*>(lds + L_MRG) + g * 132;
    float M = mrg[0];
#pragma unroll
    for (int u = 1; u < 8; ++u) M = fmaxf(M, mrg[u * 4 * 132]);
    float L = 0.f, o = 0.f;
#pragma unroll
    for (int u = 0; u < 8; ++u) {
      const float wt = __builtin_amdgcn_exp2f((mrg[u * 4 * 132] - M) * LOG2E);
      L = fmaf(wt, mrg[u * 4 * 132 + 1], L);
      o = fmaf(wt, mrg[u * 4 * 132 + 4 + d], o);
    }
    u64* pg = ws + G_P + (((size_t)ab * HQ + ahkv * 4 + g) * 4 + split) * 132;
    put_granule(pg + d, __float_as_uint(o));
    if (d == 0) { put_granule(pg + 128, __float_as_uint(M)); put_granule(pg + 129, __float_as_uint(L)); }
  }

  {
    const u64* pg = ws + G_P + ((size_t)v * HQ + hi) * 4 * 132;
    probe_wait(pg + (lane & 3) * 132 + 129, lane < 4);
    u32 o0[4], o1[4], mm[4], ll[4];
    for (u32 spins = 0; spins < SPIN_CAP; ++spins) {
      bool ok = true;
#pragma unroll
      for (int s = 0; s < 4; ++s) {
        const u64 g0 = get_granule(pg + s * 132 + 2 * lane), g1 = get_granule(pg + s * 132 + 2 * lane + 1);
        const u64 gm = get_granule(pg + s * 132 + 128), gl = get_granule(pg + s * 132 + 129);
        o0[s] = (u32)g0; o1[s] = (u32)g1; mm[s] = (u32)gm; ll[s] = (u32)gl;
        ok &= tag_ok(g0) & tag_ok(g1) & tag_ok(gm) & tag_ok(gl);
      }
      if (__all(ok)) break;
      __builtin_amdgcn_s_sleep(2);
    }
    float M = __uint_as_float(mm[0]);
#pragma unroll
    for (int s = 1; s < 4; ++s) M = fmaxf(M, __uint_as_float(mm[s]));
    float L = 0.f, a0 = 0.f, a1 = 0.f;
#pragma unroll
    for (int s = 0; s < 4; ++s) {
      const float wt = __builtin_amdgcn_exp2f((__uint_as_float(mm[s]) - M) * LOG2E);
      L = fmaf(wt, __uint_as_float(ll[s]), L);
      a0 = fmaf(wt, __uint_as_float(o0[s]), a0);
      a1 = fmaf(wt, __uint_as_float(o1[s]), a1);
    }
    const float inv = 1.0f / L;
    reinterpret_cast<u32*>(lds + L_O)[v * 64 + lane] = pack2(a0 * inv, a1 * inv);
  }
  wg_barrier();
  if (v == 0) STAMP(8);
  if (ROLE <= 1) {
    const int ct = v;
    f32x4 acc4 = {0.f, 0.f, 0.f, 0.f};
#pragma unroll
    for (int s = 0; s < 4; ++s) {
      const u32x4 af = *reinterpret_cast<const u32x4*>(lds + L_O + c * 256 + (32 * s + 8 * r) * 2);
      s16x8 bf;
#pragma unroll
      for (int jj = 0; jj < 8; ++jj)
        bf[jj] = *reinterpret_cast<const short*>(lds + L_WO + (32 * s + 8 * r + jj) * WROW + (16 * ct + c) * 2);
      acc4 = __builtin_amdgcn_mfma_f32_16x16x32_bf16(__builtin_bit_cast(s16x8, af), bf, acc4, 0, 0, 0);
    }
    if (lane < 32) {
#pragma unroll
      for (int k = 0; k < 4; ++k)
        put_granule(ws + G_S + (((size_t)cj * NB + (4 * r + k)) * 32 + hi) * 64 + 16 * ct + c, __float_as_uint(acc4[k]));
    }
  }
  if (ROLE == 2 && hi < 8 && v == 4) {
    const u64* sg = ws + G_S + ((size_t)cj * NB + hi) * 32 * 64;
    probe_wait(sg + (lane >> 1) * 64 + 63 - (lane & 1) * 32, true);
    float ssum = 0.f;
    for (u32 spins = 0; spins < SPIN_CAP; ++spins) {
      bool ok = true; ssum = 0.f;
#pragma unroll
      for (int i = 0; i < 32; ++i) { const u64 g = get_granule(sg + 64 * i + lane); ssum += __uint_as_float((u32)g); ok &= tag_ok(g); }
      if (__all(ok)) break;
      __builtin_amdgcn_s_sleep(2);
    }
    const float sb = bf16r(ssum);
    const float gate = reinterpret_cast<const float*>(lds + L_G)[lane];
    const float xo = bfu(a.x[hi * DM + 64 * cj + lane]);
    const float res = xo + bf16r(sb * gate);
    const __bf16 ob = (__bf16)res;
    a.out[hi * DM + 64 * cj + lane] = __builtin_bit_cast(u16, ob);
    STAMP(9);
  }
}

__global__ void __launch_bounds__(NTHR, 2) fused_decode_kernel(Params a) {
  __shared__ __attribute__((aligned(16))) unsigned char lds[L_END];
  const int v = __builtin_amdgcn_readfirstlane(threadIdx.x >> 6);
  if (v == 0) body<0>(a, lds, 0);
  else if (v < 4) body<1>(a, lds, v);
  else body<2>(a, lds, v);
}

__global__ void __launch_bounds__(256) zero_kernel(u32x4* p) {
  constexpr int N = (int)(G_END * 8 / 16);
  for (int i = blockIdx.x * 256 + threadIdx.x; i < N; i += 256 * 256) p[i] = u32x4{0u, 0u, 0u, 0u};
}

extern "C" void kernel_launch(void* const* d_in, const int* in_sizes, int n_in,
                              void* d_out, int out_size, void* d_ws, size_t ws_size,
                              hipStream_t stream) {
  Params p;
  p.x = (const u16*)d_in[0];
  p.kc = (const u16*)d_in[1];
  p.vc = (const u16*)d_in[2];
  p.wq = (const u16*)d_in[3];
  p.wo = (const u16*)d_in[4];
  p.w1 = (const u16*)d_in[6];
  p.b1 = (const u16*)d_in[7];
  p.w2 = (const u16*)d_in[8];
  p.b2 = (const u16*)d_in[9];
  p.out = (u16*)d_out;
  p.ws = (u64*)d_ws;
  hipLaunchKernelGGL(zero_kernel, dim3(256), dim3(256), 0, stream, (u32x4*)d_ws);
  hipLaunchKernelGGL(fused_decode_kernel, dim3(NWG), dim3(NTHR), 0, stream, p);
}
```

```cpp
#include <hip/hip_runtime.h>
#include <hip/hip_bf16.h>
#include <cstdint>
#include <cstdio>

typedef unsigned int u32;
typedef unsigned long long u64;
typedef unsigned short u16;
using u32x4 = __attribute__((ext_vector_type(4))) u32;
using f32x4 = __attribute__((ext_vector_type(4))) float;
using s16x8 = __attribute__((ext_vector_type(8))) short;
using bf16x2 = __attribute__((ext_vector_type(2))) __bf16;

constexpr int NB = 8, DM = 512, HQ = 32, HKV = 8, SK = 4096, DH = 128, FF = 1792;
constexpr int NWG = 256, NTHR = 512;
constexpr u32 TAG = 0x5A17C3E1u;
constexpr u32 SPIN_CAP = 1u << 20;
constexpr float LOG2E = 1.4426950408889634f;

constexpr size_t G_Q = 0;
constexpr size_t G_H = G_Q + (size_t)NB * 2048;
constexpr size_t G_P = G_H + (size_t)NB * 896;
constexpr size_t G_S = G_P + (size_t)NB * HQ * 4 * 132;
constexpr size_t G_Y = G_S + (size_t)8 * NB * 32 * 64;
constexpr size_t G_END = G_Y + (size_t)8 * NB * 32 * 64;

__device__ __forceinline__ float bf16r(float x) { return (float)(__bf16)x; }
__device__ __forceinline__ u32 pack2(float lo, float hi) {
  bf16x2 v = {(__bf16)lo, (__bf16)hi};
  return __builtin_bit_cast(u32, v);
}
__device__ __forceinline__ float bflo(u32 x) { return __builtin_bit_cast(float, x << 16); }
__device__ __forceinline__ float bfhi(u32 x) { return __builtin_bit_cast(float, x & 0xffff0000u); }
__device__ __forceinline__ float bfu(u16 x) { return __builtin_bit_cast(float, (u32)x << 16); }
__device__ __forceinline__ float dot2(u32 k, u32 q, float acc) {
  return __builtin_amdgcn_fdot2_f32_bf16(__builtin_bit_cast(bf16x2, k), __builtin_bit_cast(bf16x2, q), acc, false);
}
template <int CTRL> __device__ __forceinline__ float dpp(float x) {
  return __builtin_bit_cast(float, __builtin_amdgcn_mov_dpp(__builtin_bit_cast(int, x), CTRL, 0xf, 0xf, true));
}
constexpr int XOR1 = 0xB1, XOR2 = 0x4E, XOR7 = 0x141, XOR8 = 0x128, ROR4 = 0x124;
__device__ __forceinline__ float xrow16_max(float x) {
  auto s = __builtin_amdgcn_permlane16_swap(__float_as_uint(x), __float_as_uint(x), false, false);
  x = fmaxf(__uint_as_float(s[0]), __uint_as_float(s[1]));
  auto t = __builtin_amdgcn_permlane32_swap(__float_as_uint(x), __float_as_uint(x), false, false);
  return fmaxf(__uint_as_float(t[0]), __uint_as_float(t[1]));
}
__device__ __forceinline__ float xrow16_sum(float x) {
  auto s = __builtin_amdgcn_permlane16_swap(__float_as_uint(x), __float_as_uint(x), false, false);
  x = __uint_as_float(s[0]) + __uint_as_float(s[1]);
  auto t = __builtin_amdgcn_permlane32_swap(__float_as_uint(x), __float_as_uint(x), false, false);
  return __uint_as_float(t[0]) + __uint_as_float(t[1]);
}
__device__ __forceinline__ void put_granule(u64* g, u32 value) {
  __hip_atomic_store(g, ((u64)TAG << 32) | (u64)value, __ATOMIC_RELAXED, __HIP_MEMORY_SCOPE_AGENT);
}
__device__ __forceinline__ u64 get_granule(const u64* g) {
  return __hip_atomic_load(g, __ATOMIC_RELAXED, __HIP_MEMORY_SCOPE_AGENT);
}
__device__ __forceinline__ bool tag_ok(u64 g) { return (u32)(g >> 32) == TAG; }
__device__ __forceinline__ void wg_barrier() {
  asm volatile("s_waitcnt lgkmcnt(0)" ::: "memory");
  __builtin_amdgcn_s_barrier();
  asm volatile("" ::: "memory");
}
__device__ __forceinline__ u32x4 ld_nt(const u16* p) {
  return __builtin_nontemporal_load(reinterpret_cast<const u32x4*>(p));
}
__device__ __forceinline__ u32 lds_flag_load(u32* f) {
  return (u32)__builtin_amdgcn_readfirstlane(__hip_atomic_load(f, __ATOMIC_RELAXED, __HIP_MEMORY_SCOPE_WORKGROUP));
}
__device__ __forceinline__ void lds_wait_ge(u32* f, u32 want) {
  for (u32 spins = 0; spins < SPIN_CAP * 8u; ++spins) {
    if (lds_flag_load(f) >= want) break;
    __builtin_amdgcn_s_sleep(1);
  }
  asm volatile("" ::: "memory");
}
__device__ __forceinline__ void probe_wait(const u64* g, bool active) {
  for (u32 spins = 0; spins < SPIN_CAP; ++spins) {
    bool ok = true;
    if (active) ok = tag_ok(get_granule(g));
    if (__all(ok)) break;
    __builtin_amdgcn_s_sleep(2);
  }
}

#define STAMP(k) do { } while (0)

struct Params {
  const u16* x; const u16* kc; const u16* vc; const u16* wq; const u16* wo;
  const u16* w1; const u16* b1; const u16* w2; const u16* b2;
  u16* out; u64* ws;
};

constexpr int WROW = 144;
constexpr int L_WO = 0;
constexpr int L_W2 = L_WO + 128 * WROW;
constexpr int L_SVC = L_W2 + 64 * WROW;
constexpr int L_Q = L_SVC + 4 * 2 * 8 * 16 * 4;
constexpr int L_H = L_Q + 1024;
constexpr int L_O = L_H + 16 * 32 * 4;
constexpr int L_G = L_O + 16 * 64 * 4;
constexpr int L_FLAG = L_G + 256;
constexpr int L_P = L_FLAG + 32;
constexpr int L_MRG = L_P + 8 * 144 * 4 * 4;
constexpr int L_WQS = L_MRG + 8 * 4 * 132 * 4;
constexpr int L_W1S = L_WQS + 4 * 4352;
constexpr int L_XN = L_W1S + 4 * 2304;
constexpr int L_END = L_XN + 64;

template <int ROLE>
__device__ __forceinline__ void body(const Params& a, unsigned char* lds, const int v) {
  constexpr int NG = (ROLE == 0) ? 1 : 9;
  const int w = blockIdx.x;
  const int tid = threadIdx.x, lane = tid & 63;
  const int r = lane >> 4, c = lane & 15;
  const int bh = w >> 2, split = w & 3;
  const int ab = bh >> 3, ahkv = bh & 7;
  const int hi = w >> 3, cj = w & 7;
  const int qb = (w & 7) * 32 + (w >> 3);
  const bool h_on = (qb < 224);
  u64* ws = a.ws;
  u32* flags = reinterpret_cast<u32*>(lds + L_FLAG);
  const u64 t_start = __builtin_amdgcn_s_memrealtime();

  if (ROLE == 0) {
    if (lane < 8) flags[lane] = 0u;
  }
  wg_barrier();

  const int key0 = (ROLE == 0) ? 0 : 16 + 144 * (v - 1);
  const size_t kvbase = ((size_t)bh * SK + split * 1024 + key0) * DH + (size_t)r * DH + 8 * c;
  u32x4 kv[4 * NG];
  u32 b1v = 0;

  if (ROLE <= 1) {
    u32x4 xs[2], xa[4];
#pragma unroll
    for (int i = 0; i < 2; ++i) xs[i] = ld_nt(a.x + (2 * v + i) * DM + 8 * lane);
#pragma unroll
    for (int s = 0; s < 4; ++s) xa[s] = ld_nt(a.x + (c & 7) * DM + 128 * v + 32 * s + 8 * r);
    u32x4 wqr[4], w1r[2];
#pragma unroll
    for (int u = 0; u < 4; ++u)
      wqr[u] = ld_nt(a.wq + (size_t)(128 * v + 32 * u + (lane >> 1)) * 4096 + 16 * qb + 8 * (lane & 1));
#pragma unroll
    for (int u = 0; u < 2; ++u)
      w1r[u] = ld_nt(a.w1 + (size_t)(128 * v + 64 * u + lane) * FF + 8 * (h_on ? qb : 0));
    if (ROLE == 0) b1v = *reinterpret_cast<const u32*>(a.b1 + 8 * (h_on ? qb : 0) + 2 * (lane & 3));
    __builtin_amdgcn_sched_barrier(0);
    if (lane == 0) __hip_atomic_fetch_add(flags + 3, 1u, __ATOMIC_RELAXED, __HIP_MEMORY_SCOPE_WORKGROUP);
    float* stat = reinterpret_cast<float*>(lds + L_XN);
#pragma unroll
    for (int i = 0; i < 2; ++i) {
      float s = 0.f;
#pragma unroll
      for (int k = 0; k < 4; ++k) s += bflo(xs[i][k]) + bfhi(xs[i][k]);
      s += dpp<XOR1>(s); s += dpp<XOR2>(s); s += dpp<XOR7>(s); s += dpp<XOR8>(s); s = xrow16_sum(s);
      const float mean = s * (1.0f / DM);
      float q2 = 0.f;
#pragma unroll
      for (int k = 0; k < 4; ++k) {
        const float d0 = bflo(xs[i][k]) - mean, d1 = bfhi(xs[i][k]) - mean;
        q2 = fmaf(d0, d0, q2); q2 = fmaf(d1, d1, q2);
      }
      q2 += dpp<XOR1>(q2); q2 += dpp<XOR2>(q2); q2 += dpp<XOR7>(q2); q2 += dpp<XOR8>(q2); q2 = xrow16_sum(q2);
      const float var_bf = bf16r(q2 * (1.0f / DM));
      if (lane == 0) { stat[4 * v + 2 * i] = bf16r(mean); stat[4 * v + 2 * i + 1] = bf16r(sqrtf(bf16r(var_bf + 1e-5f))); }
    }
    asm volatile("s_waitcnt lgkmcnt(0)" ::: "memory");
    if (lane == 0) __hip_atomic_fetch_add(flags + 4, 1u, __ATOMIC_RELAXED, __HIP_MEMORY_SCOPE_WORKGROUP);
    if (v == 1) STAMP(12);
    unsigned char* wqs = lds + L_WQS + v * 4352;
    unsigned char* w1s = lds + L_W1S + v * 2304;
#pragma unroll
    for (int u = 0; u < 4; ++u) {
      const int rr = 32 * u + (lane >> 1);
      *reinterpret_cast<u32x4*>(wqs + rr * 32 + (rr >> 3) * 16 + 16 * (lane & 1)) = wqr[u];
    }
#pragma unroll
    for (int u = 0; u < 2; ++u) {
      const int rr = 64 * u + lane;
      *reinterpret_cast<u32x4*>(w1s + rr * 16 + (rr >> 3) * 16) = w1r[u];
    }
    lds_wait_ge(flags + 4, 4u);
    const float mean_bf = stat[2 * (c & 7)], sd_bf = stat[2 * (c & 7) + 1];
    asm volatile("s_waitcnt lgkmcnt(0)" ::: "memory");
    f32x4 accq = {0.f, 0.f, 0.f, 0.f}, acch = {0.f, 0.f, 0.f, 0.f};
#pragma unroll
    for (int s = 0; s < 4; ++s) {
      u32x4 af;
#pragma unroll
      for (int k = 0; k < 4; ++k)
        af[k] = (c < 8) ? pack2(bf16r(bflo(xa[s][k]) - mean_bf) / sd_bf, bf16r(bfhi(xa[s][k]) - mean_bf) / sd_bf) : 0u;
      const s16x8 afr = __builtin_bit_cast(s16x8, af);
      s16x8 bq, bws;
#pragma unroll
      for (int jj = 0; jj < 8; ++jj) {
        const int k = 32 * s + 8 * r + jj;
        bq[jj] = *reinterpret_cast<const short*>(wqs + k * 32 + (k >> 3) * 16 + c * 2);
        bws[jj] = *reinterpret_cast<const short*>(w1s + k * 16 + (k >> 3) * 16 + (c & 7) * 2);
      }
      if (c >= 8) bws = s16x8{0, 0, 0, 0, 0, 0, 0, 0};
      accq = __builtin_amdgcn_mfma_f32_16x16x32_bf16(afr, bq, accq, 0, 0, 0);
      acch = __builtin_amdgcn_mfma_f32_16x16x32_bf16(afr, bws, acch, 0, 0, 0);
    }
    if (v == 1) STAMP(13);
    float* svc = reinterpret_cast<float*>(lds + L_SVC) + v * 256;
    if (lane < 32) {
#pragma unroll
      for (int k = 0; k < 4; ++k) {
        svc[(4 * r + k) * 16 + c] = accq[k];
        svc[128 + (4 * r + k) * 16 + c] = acch[k];
      }
    }
    if (ROLE == 1) {
      asm volatile("s_waitcnt lgkmcnt(0)" ::: "memory");
      if (lane == 0) __hip_atomic_fetch_add(flags + 1, 1u, __ATOMIC_RELAXED, __HIP_MEMORY_SCOPE_WORKGROUP);
    }
  }
  STAMP(24 + v);

  if (ROLE == 2) {
    const int t4 = tid - 256;
    lds_wait_ge(flags + 3, 4u);
    u32x4 st[6];
#pragma unroll
    for (int u = 0; u < 4; ++u) {
      const int e = t4 + 256 * u, row = e >> 3, seg = e & 7;
      st[u] = ld_nt(a.wo + (size_t)(128 * hi + row) * DM + 64 * cj + 8 * seg);
    }
#pragma unroll
    for (int u = 0; u < 2; ++u) {
      const int e = t4 + 256 * u, row = e >> 3, seg = e & 7;
      st[4 + u] = ld_nt(a.w2 + (size_t)(56 * hi + (row < 56 ? row : 0)) * DM + 64 * cj + 8 * seg);
    }
#pragma unroll
    for (int i = 0; i < 4 * NG; ++i) kv[i] = ld_nt(a.kc + kvbase + (size_t)i * 4 * DH);
    __builtin_amdgcn_sched_barrier(0);
#pragma unroll
    for (int u = 0; u < 4; ++u) {
      const int e = t4 + 256 * u, row = e >> 3, seg = e & 7;
      *reinterpret_cast<u32x4*>(lds + L_WO + row * WROW + 16 * seg) = st[u];
    }
#pragma unroll
    for (int u = 0; u < 2; ++u) {
      const int e = t4 + 256 * u, row = e >> 3, seg = e & 7;
      *reinterpret_cast<u32x4*>(lds + L_W2 + row * WROW + 16 * seg) = row < 56 ? st[4 + u] : u32x4{0u, 0u, 0u, 0u};
    }
    reinterpret_cast<u32*>(lds + L_H)[256 + t4] = 0u;
    reinterpret_cast<u32*>(lds + L_O)[512 + t4] = 0u;
    reinterpret_cast<u32*>(lds + L_O)[768 + t4] = 0u;
    asm volatile("s_waitcnt lgkmcnt(0)" ::: "memory");
    if (lane == 0) __hip_atomic_fetch_add(flags + 2, 1u, __ATOMIC_RELAXED, __HIP_MEMORY_SCOPE_WORKGROUP);
  }
  if (ROLE == 1) {
#pragma unroll
    for (int i = 0; i < 4 * NG; ++i) kv[i] = ld_nt(a.kc + kvbase + (size_t)i * 4 * DH);
    __builtin_amdgcn_sched_barrier(0);
  }

  if (ROLE == 0) {
    lds_wait_ge(flags + 1, 3u);
    const float* svc = reinterpret_cast<const float*>(lds + L_SVC);
    {
      const int b = lane >> 3, cp = lane & 7;
      float s0 = 0.f, s1 = 0.f;
#pragma unroll
      for (int u = 0; u < 4; ++u) { s0 += svc[u * 256 + b * 16 + 2 * cp]; s1 += svc[u * 256 + b * 16 + 2 * cp + 1]; }
      put_granule(ws + G_Q + (size_t)b * 2048 + 8 * qb + cp, pack2(s0, s1));
    }
    if (h_on && lane < 32) {
      const int b = lane >> 2, cp = lane & 3;
      float hv[2];
#pragma unroll
      for (int e = 0; e < 2; ++e) {
        float s0 = 0.f;
#pragma unroll
        for (int u = 0; u < 4; ++u) s0 += svc[u * 256 + 128 + b * 16 + 2 * cp + e];
        const float t = bf16r(bf16r(s0) + (e ? bfhi(b1v) : bflo(b1v)));
        const float inner = 0.7978845608028654f * (t + 0.044715f * t * t * t);
        hv[e] = 0.5f * t * (1.0f + tanhf(inner));
      }
      put_granule(ws + G_H + (size_t)b * 896 + 4 * qb + cp, pack2(hv[0], hv[1]));
    }
    STAMP(1);
    const u64* qg = ws + G_Q + (size_t)ab * 2048 + ahkv * 256 + lane;
    u32 val[4], hval[4];
    const u64* hp[4];
    int hidx[4];
#pragma unroll
    for (int k = 0; k < 4; ++k) {
      const int idx = min(lane + 64 * k, 223);
      const int b = idx / 28, t = idx % 28;
      hp[k] = ws + G_H + (size_t)b * 896 + 28 * hi + t;
      hidx[k] = b * 32 + t;
    }
    for (u32 spins = 0; spins < SPIN_CAP; ++spins) {
      bool ok = true;
#pragma unroll
      for (int k = 0; k < 4; ++k) { const u64 g = get_granule(qg + 64 * k); val[k] = (u32)g; ok &= tag_ok(g); }
      if (__all(ok)) break;
      __builtin_amdgcn_s_sleep(1);
    }
    u32* qs = reinterpret_cast<u32*>(lds + L_Q);
#pragma unroll
    for (int k = 0; k < 4; ++k) qs[64 * k + lane] = val[k];
    asm volatile("s_waitcnt lgkmcnt(0)" ::: "memory");
    if (lane == 0) __hip_atomic_store(flags, 1u, __ATOMIC_RELAXED, __HIP_MEMORY_SCOPE_WORKGROUP);
    STAMP(2);
#pragma unroll
    for (int i = 0; i < 4 * NG; ++i) kv[i] = ld_nt(a.kc + kvbase + (size_t)i * 4 * DH);
    for (u32 spins = 0; spins < SPIN_CAP; ++spins) {
      bool ok = true;
#pragma unroll
      for (int k = 0; k < 4; ++k) { const u64 g = get_granule(hp[k]); hval[k] = (u32)g; ok &= tag_ok(g); }
      if (__all(ok)) break;
      __builtin_amdgcn_s_sleep(1);
    }
    u32* hs = reinterpret_cast<u32*>(lds + L_H);
#pragma unroll
    for (int k = 0; k < 4; ++k) if (lane + 64 * k < 224) hs[hidx[k]] = hval[k];
    if (lane < 32) hs[(lane >> 2) * 32 + 28 + (lane & 3)] = 0u;
    lds_wait_ge(flags + 2, 4u);
    asm volatile("s_waitcnt lgkmcnt(0)" ::: "memory");
#pragma unroll
    for (int ct = 0; ct < 4; ++ct) {
      f32x4 acc = {0.f, 0.f, 0.f, 0.f};
#pragma unroll
      for (int s = 0; s < 2; ++s) {
        const u32x4 af = *reinterpret_cast<const u32x4*>(lds + L_H + c * 128 + (32 * s + 8 * r) * 2);
        s16x8 bf;
#pragma unroll
        for (int jj = 0; jj < 8; ++jj)
          bf[jj] = *reinterpret_cast<const short*>(lds + L_W2 + (32 * s + 8 * r + jj) * WROW + (16 * ct + c) * 2);
        acc = __builtin_amdgcn_mfma_f32_16x16x32_bf16(__builtin_bit_cast(s16x8, af), bf, acc, 0, 0, 0);
      }
      if (lane < 32) {
#pragma unroll
        for (int k = 0; k < 4; ++k)
          put_granule(ws + G_Y + (((size_t)cj * NB + (4 * r + k)) * 32 + hi) * 64 + 16 * ct + c, __float_as_uint(acc[k]));
      }
    }
    STAMP(3);
  } else {
    lds_wait_ge(flags, 1u);
  }

  u32x4 qpk[4];
#pragma unroll
  for (int g = 0; g < 4; ++g) qpk[g] = *reinterpret_cast<const u32x4*>(lds + L_Q + g * 256 + c * 16);
  float sc[NG];
#pragma unroll
  for (int grp = 0; grp < NG; ++grp) {
    float x[16];
#pragma unroll
    for (int t = 0; t < 4; ++t)
#pragma unroll
      for (int g = 0; g < 4; ++g) {
        float d = 0.f;
#pragma unroll
        for (int k = 0; k < 4; ++k) d = dot2(kv[4 * grp + t][k], qpk[g][k], d);
        x[t * 4 + g] = d;
      }
#define TR_STEP(HALF, CTRL, BIT) _Pragma("unroll") for (int h = 0; h < (HALF); ++h) { \
      const float lo_ = x[h], hi_ = x[h + (HALF)]; const float t1_ = lo_ + dpp<CTRL>(lo_), t2_ = hi_ + dpp<CTRL>(hi_); \
      x[h] = (c & (BIT)) ? t2_ : t1_; }
    TR_STEP(8, XOR8, 8) TR_STEP(4, XOR7, 4) TR_STEP(2, XOR2, 2) TR_STEP(1, XOR1, 1)
#undef TR_STEP
    sc[grp] = bf16r(bf16r(x[0]) * 0.08838834764831845f);
#pragma unroll
    for (int t = 0; t < 4; ++t) kv[4 * grp + t] = ld_nt(a.vc + kvbase + (size_t)(4 * grp + t) * 4 * DH);
    __builtin_amdgcn_sched_barrier(0);
  }
  STAMP(48 + v);
  float m = sc[0];
#pragma unroll
  for (int grp = 1; grp < NG; ++grp) m = fmaxf(m, sc[grp]);
  m = fmaxf(m, dpp<ROR4>(m)); m = fmaxf(m, dpp<XOR8>(m)); m = xrow16_max(m);
  float l = 0.f;
  {
    float* pl = reinterpret_cast<float*>(lds + L_P) + v * 576;
#pragma unroll
    for (int grp = 0; grp < NG; ++grp) {
      const float p = __builtin_amdgcn_exp2f((sc[grp] - m) * LOG2E);
      l += p;
      pl[(16 * grp + 4 * (c >> 2) + r) * 4 + (c & 3)] = p;
    }
  }
  l += dpp<ROR4>(l); l += dpp<XOR8>(l); l = xrow16_sum(l);
  asm volatile("s_waitcnt lgkmcnt(0)" ::: "memory");

  float acc[32];
#pragma unroll
  for (int k = 0; k < 32; ++k) acc[k] = 0.f;
  {
    const f32x4* pl4 = reinterpret_cast<const f32x4*>(lds + L_P) + v * 144;
#pragma unroll
    for (int i = 0; i < 4 * NG; ++i) {
      const f32x4 p = pl4[4 * i + r];
      float vf[8];
#pragma unroll
      for (int k = 0; k < 4; ++k) { vf[2 * k] = bflo(kv[i][k]); vf[2 * k + 1] = bfhi(kv[i][k]); }
#pragma unroll
      for (int g = 0; g < 4; ++g)
#pragma unroll
        for (int k = 0; k < 8; ++k) acc[g * 8 + k] = fmaf(p[g], vf[k], acc[g * 8 + k]);
    }
  }
#pragma unroll
  for (int k = 0; k < 32; ++k) acc[k] = xrow16_sum(acc[k]);
  STAMP(40 + v);
  {
    float* mrg = reinterpret_cast<float*>(lds + L_MRG) + v * 4 * 132;
    if (r == 0) {
#pragma unroll
      for (int g = 0; g < 4; ++g) {
        *reinterpret_cast<f32x4*>(mrg + g * 132 + 4 + 8 * c) = f32x4{acc[g * 8], acc[g * 8 + 1], acc[g * 8 + 2], acc[g * 8 + 3]};
        *reinterpret_cast<f32x4*>(mrg + g * 132 + 4 + 8 * c + 4) = f32x4{acc[g * 8 + 4], acc[g * 8 + 5], acc[g * 8 + 6], acc[g * 8 + 7]};
      }
      if (c < 4) { mrg[c * 132] = m; mrg[c * 132 + 1] = l; }
    }
  }
  if (ROLE == 0 && hi < 8) {
    const u64* yg = ws + G_Y + ((size_t)cj * NB + hi) * 32 * 64 + lane;
    float ysum = 0.f;
    for (u32 spins = 0; spins < SPIN_CAP; ++spins) {
      bool ok = true; ysum = 0.f;
#pragma unroll
      for (int i = 0; i < 32; ++i) { const u64 g = get_granule(yg + 64 * i); ysum += __uint_as_float((u32)g); ok &= tag_ok(g); }
      if (__all(ok)) break;
      __builtin_amdgcn_s_sleep(8);
    }
    const float y = bf16r(bf16r(ysum) + bfu(a.b2[64 * cj + lane]));
    const float gate = bf16r(y / (1.0f + __expf(-y)));
    reinterpret_cast<float*>(lds + L_G)[lane] = gate;
  }
  wg_barrier();
  if (v == 0) STAMP(7);

  {
    const int g = tid >> 7, d = tid & 127;
    const float* mrg = reinterpret_cast<const float*>(lds + L_MRG) + g * 132;
    float M = mrg[0];
#pragma unroll
    for (int u = 1; u < 8; ++u) M = fmaxf(M, mrg[u * 4 * 132]);
    float L = 0.f, o = 0.f;
#pragma unroll
    for (int u = 0; u < 8; ++u) {
      const float wt = __builtin_amdgcn_exp2f((mrg[u * 4 * 132] - M) * LOG2E);
      L = fmaf(wt, mrg[u * 4 * 132 + 1], L);
      o = fmaf(wt, mrg[u * 4 * 132 + 4 + d], o);
    }
    u64* pg = ws + G_P + (((size_t)ab * HQ + ahkv * 4 + g) * 4 + split) * 132;
    put_granule(pg + d, __float_as_uint(o));
    if (d == 0) { put_granule(pg + 128, __float_as_uint(M)); put_granule(pg + 129, __float_as_uint(L)); }
  }

  {
    const u64* pg = ws + G_P + ((size_t)v * HQ + hi) * 4 * 132;
    probe_wait(pg + (lane & 3) * 132 + 129, lane < 4);
    u32 o0[4], o1[4], mm[4], ll[4];
    for (u32 spins = 0; spins < SPIN_CAP; ++spins) {
      bool ok = true;
#pragma unroll
      for (int s = 0; s < 4; ++s) {
        const u64 g0 = get_granule(pg + s * 132 + 2 * lane), g1 = get_granule(pg + s * 132 + 2 * lane + 1);
        const u64 gm = get_granule(pg + s * 132 + 128), gl = get_granule(pg + s * 132 + 129);
        o0[s] = (u32)g0; o1[s] = (u32)g1; mm[s] = (u32)gm; ll[s] = (u32)gl;
        ok &= tag_ok(g0) & tag_ok(g1) & tag_ok(gm) & tag_ok(gl);
      }
      if (__all(ok)) break;
      __builtin_amdgcn_s_sleep(2);
    }
    float M = __uint_as_float(mm[0]);
#pragma unroll
    for (int s = 1; s < 4; ++s) M = fmaxf(M, __uint_as_float(mm[s]));
    float L = 0.f, a0 = 0.f, a1 = 0.f;
#pragma unroll
    for (int s = 0; s < 4; ++s) {
      const float wt = __builtin_amdgcn_exp2f((__uint_as_float(mm[s]) - M) * LOG2E);
      L = fmaf(wt, __uint_as_float(ll[s]), L);
      a0 = fmaf(wt, __uint_as_float(o0[s]), a0);
      a1 = fmaf(wt, __uint_as_float(o1[s]), a1);
    }
    const float inv = 1.0f / L;
    reinterpret_cast<u32*>(lds + L_O)[v * 64 + lane] = pack2(a0 * inv, a1 * inv);
  }
  wg_barrier();
  if (v == 0) STAMP(8);
  if (ROLE <= 1) {
    const int ct = v;
    f32x4 acc4 = {0.f, 0.f, 0.f, 0.f};
#pragma unroll
    for (int s = 0; s < 4; ++s) {
      const u32x4 af = *reinterpret_cast<const u32x4*>(lds + L_O + c * 256 + (32 * s + 8 * r) * 2);
      s16x8 bf;
#pragma unroll
      for (int jj = 0; jj < 8; ++jj)
        bf[jj] = *reinterpret_cast<const short*>(lds + L_WO + (32 * s + 8 * r + jj) * WROW + (16 * ct + c) * 2);
      acc4 = __builtin_amdgcn_mfma_f32_16x16x32_bf16(__builtin_bit_cast(s16x8, af), bf, acc4, 0, 0, 0);
    }
    if (lane < 32) {
#pragma unroll
      for (int k = 0; k < 4; ++k)
        put_granule(ws + G_S + (((size_t)cj * NB + (4 * r + k)) * 32 + hi) * 64 + 16 * ct + c, __float_as_uint(acc4[k]));
    }
  }
  if (ROLE == 2 && hi < 8 && v == 4) {
    const u64* sg = ws + G_S + ((size_t)cj * NB + hi) * 32 * 64;
    probe_wait(sg + (lane >> 1) * 64 + 63 - (lane & 1) * 32, true);
    float ssum = 0.f;
    for (u32 spins = 0; spins < SPIN_CAP; ++spins) {
      bool ok = true; ssum = 0.f;
#pragma unroll
      for (int i = 0; i < 32; ++i) { const u64 g = get_granule(sg + 64 * i + lane); ssum += __uint_as_float((u32)g); ok &= tag_ok(g); }
      if (__all(ok)) break;
      __builtin_amdgcn_s_sleep(2);
    }
    const float sb = bf16r(ssum);
    const float gate = reinterpret_cast<const float*>(lds + L_G)[lane];
    const float xo = bfu(a.x[hi * DM + 64 * cj + lane]);
    const float res = xo + bf16r(sb * gate);
    const __bf16 ob = (__bf16)res;
    a.out[hi * DM + 64 * cj + lane] = __builtin_bit_cast(u16, ob);
    if (lane == 0 && (w == 0 || w == 63)) printf("wg %d in-kernel %.2f us\n", w, (double)(__builtin_amdgcn_s_memrealtime() - t_start) * 0.01);
  }
}

__global__ void __launch_bounds__(NTHR, 2) fused_decode_kernel(Params a) {
  __shared__ __attribute__((aligned(16))) unsigned char lds[L_END];
  const int v = __builtin_amdgcn_readfirstlane(threadIdx.x >> 6);
  if (v == 0) body<0>(a, lds, 0);
  else if (v < 4) body<1>(a, lds, v);
  else body<2>(a, lds, v);
}

extern "C" void kernel_launch(void* const* d_in, const int* in_sizes, int n_in,
                              void* d_out, int out_size, void* d_ws, size_t ws_size,
                              hipStream_t stream) {
  Params p;
  p.x = (const u16*)d_in[0];
  p.kc = (const u16*)d_in[1];
  p.vc = (const u16*)d_in[2];
  p.wq = (const u16*)d_in[3];
  p.wo = (const u16*)d_in[4];
  p.w1 = (const u16*)d_in[6];
  p.b1 = (const u16*)d_in[7];
  p.w2 = (const u16*)d_in[8];
  p.b2 = (const u16*)d_in[9];
  p.out = (u16*)d_out;
  p.ws = (u64*)d_ws;
  (void)hipMemsetAsync(d_ws, 0, G_END * 8, stream);
  hipLaunchKernelGGL(fused_decode_kernel, dim3(NWG), dim3(NTHR), 0, stream, p);
}
```

```cpp
#include <hip/hip_runtime.h>
#include <hip/hip_bf16.h>
#include <cstdint>

typedef unsigned int u32;
typedef unsigned long long u64;
typedef unsigned short u16;
using u32x4 = __attribute__((ext_vector_type(4))) u32;
using f32x4 = __attribute__((ext_vector_type(4))) float;
using s16x8 = __attribute__((ext_vector_type(8))) short;
using bf16x2 = __attribute__((ext_vector_type(2))) __bf16;

constexpr int NB = 8, DM = 512, HQ = 32, HKV = 8, SK = 4096, DH = 128, FF = 1792;
constexpr int NWG = 256, NTHR = 512;
constexpr u32 SPIN_CAP = 1u << 20;
constexpr float LOG2E = 1.4426950408889634f;

constexpr u32 G_Q = 16;
constexpr u32 G_H = G_Q + NB * 1024;
constexpr u32 G_P = G_H + NB * 448;
constexpr u32 G_S = G_P + NB * HQ * 4 * 66;
constexpr u32 G_Y = G_S + 8 * NB * 32 * 32;
constexpr u32 G_END = G_Y + 8 * NB * 32 * 32;

__device__ __forceinline__ float bf16r(float x) { return (float)(__bf16)x; }
__device__ __forceinline__ u32 pack2(float lo, float hi) {
  bf16x2 v = {(__bf16)lo, (__bf16)hi};
  return __builtin_bit_cast(u32, v);
}
__device__ __forceinline__ float bflo(u32 x) { return __builtin_bit_cast(float, x << 16); }
__device__ __forceinline__ float bfhi(u32 x) { return __builtin_bit_cast(float, x & 0xffff0000u); }
__device__ __forceinline__ float bfu(u16 x) { return __builtin_bit_cast(float, (u32)x << 16); }
__device__ __forceinline__ float dot2(u32 k, u32 q, float acc) {
  return __builtin_amdgcn_fdot2_f32_bf16(__builtin_bit_cast(bf16x2, k), __builtin_bit_cast(bf16x2, q), acc, false);
}
template <int CTRL> __device__ __forceinline__ float dpp(float x) {
  return __builtin_bit_cast(float, __builtin_amdgcn_mov_dpp(__builtin_bit_cast(int, x), CTRL, 0xf, 0xf, true));
}
constexpr int XOR1 = 0xB1, XOR2 = 0x4E, XOR7 = 0x141, XOR8 = 0x128, ROR4 = 0x124;
__device__ __forceinline__ float xrow16_max(float x) {
  auto s = __builtin_amdgcn_permlane16_swap(__float_as_uint(x), __float_as_uint(x), false, false);
  x = fmaxf(__uint_as_float(s[0]), __uint_as_float(s[1]));
  auto t = __builtin_amdgcn_permlane32_swap(__float_as_uint(x), __float_as_uint(x), false, false);
  return fmaxf(__uint_as_float(t[0]), __uint_as_float(t[1]));
}
__device__ __forceinline__ float xrow16_sum(float x) {
  auto s = __builtin_amdgcn_permlane16_swap(__float_as_uint(x), __float_as_uint(x), false, false);
  x = __uint_as_float(s[0]) + __uint_as_float(s[1]);
  auto t = __builtin_amdgcn_permlane32_swap(__float_as_uint(x), __float_as_uint(x), false, false);
  return __uint_as_float(t[0]) + __uint_as_float(t[1]);
}
typedef __amdgpu_buffer_rsrc_t rsrc_t;
struct Tag { u32 lo, hi; };
__device__ __forceinline__ void put_granule(rsrc_t rs, u32 g, u32 v0, u32 v1, Tag t) {
  __builtin_amdgcn_raw_buffer_store_b128(u32x4{v0, v1, t.lo, t.hi}, rs, (int)(g * 16u), 0, 16);
}
__device__ __forceinline__ u32x4 get_granule(rsrc_t rs, u32 g) {
  return __builtin_amdgcn_raw_buffer_load_b128(rs, (int)(g * 16u), 0, 16);
}
__device__ __forceinline__ bool tag_ok(u32x4 g, Tag t) { return (g[2] == t.lo) & (g[3] == t.hi); }
__device__ __forceinline__ void wg_barrier() {
  asm volatile("s_waitcnt lgkmcnt(0)" ::: "memory");
  __builtin_amdgcn_s_barrier();
  asm volatile("" ::: "memory");
}
__device__ __forceinline__ u32x4 ld_nt(const u16* p) {
  return __builtin_nontemporal_load(reinterpret_cast<const u32x4*>(p));
}
__device__ __forceinline__ u32 lds_flag_load(u32* f) {
  return (u32)__builtin_amdgcn_readfirstlane(__hip_atomic_load(f, __ATOMIC_RELAXED, __HIP_MEMORY_SCOPE_WORKGROUP));
}
__device__ __forceinline__ void lds_wait_ge(u32* f, u32 want) {
  for (u32 spins = 0; spins < SPIN_CAP * 8u; ++spins) {
    if (lds_flag_load(f) >= want) break;
    __builtin_amdgcn_s_sleep(1);
  }
  asm volatile("" ::: "memory");
}
__device__ __forceinline__ void probe_wait(rsrc_t rs, u32 g, bool active, Tag t) {
  for (u32 spins = 0; spins < SPIN_CAP; ++spins) {
    asm volatile("" ::: "memory");
    bool ok = true;
    if (active) ok = tag_ok(get_granule(rs, g), t);
    if (__all(ok)) break;
    __builtin_amdgcn_s_sleep(2);
  }
}

#define STAMP(k) do { } while (0)

struct Params {
  const u16* x; const u16* kc; const u16* vc; const u16* wq; const u16* wo;
  const u16* w1; const u16* b1; const u16* w2; const u16* b2;
  u16* out; u64* ws;
};

constexpr int WROW = 144;
constexpr int L_WO = 0;
constexpr int L_W2 = L_WO + 128 * WROW;
constexpr int L_SVC = L_W2 + 64 * WROW;
constexpr int L_Q = L_SVC + 4 * 2 * 8 * 16 * 4;
constexpr int L_H = L_Q + 1024;
constexpr int L_O = L_H + 16 * 32 * 4;
constexpr int L_G = L_O + 16 * 64 * 4;
constexpr int L_FLAG = L_G + 256;
constexpr int L_P = L_FLAG + 32;
constexpr int L_MRG = L_P + 8 * 144 * 4 * 4;
constexpr int L_WQS = L_MRG + 8 * 4 * 132 * 4;
constexpr int L_W1S = L_WQS + 4 * 4352;
constexpr int L_XN = L_W1S + 4 * 2304;
constexpr int L_END = L_XN + 64;

template <int ROLE>
__device__ __forceinline__ void body(const Params& a, unsigned char* lds, const int v) {
  constexpr int NG = (ROLE == 0) ? 1 : 9;
  const int w = blockIdx.x;
  const int tid = threadIdx.x, lane = tid & 63;
  const int r = lane >> 4, c = lane & 15;
  const int bh = w >> 2, split = w & 3;
  const int ab = bh >> 3, ahkv = bh & 7;
  const int hi = w >> 3, cj = w & 7;
  const int qb = (w & 7) * 32 + (w >> 3);
  const bool h_on = (qb < 224);
  const rsrc_t rs = __builtin_amdgcn_make_buffer_rsrc((void*)a.ws, 0, (int)(G_END * 16u), 0x00020000);
  Tag tag;
  {
    const u64 e = __hip_atomic_load(a.ws, __ATOMIC_RELAXED, __HIP_MEMORY_SCOPE_AGENT) + 1ull;
    tag.lo = (u32)__builtin_amdgcn_readfirstlane((u32)e);
    tag.hi = (u32)__builtin_amdgcn_readfirstlane((u32)(e >> 32));
  }
  u32* flags = reinterpret_cast<u32*>(lds + L_FLAG);
  STAMP(16 + v);

  if (ROLE == 0) {
    if (lane < 8) flags[lane] = 0u;
  }
  wg_barrier();

  const int key0 = (ROLE == 0) ? 0 : 16 + 144 * (v - 1);
  const size_t kvbase = ((size_t)bh * SK + split * 1024 + key0) * DH + (size_t)r * DH + 8 * c;
  u32x4 kv[4 * NG];
  u32 b1v = 0;

  if (ROLE <= 1) {
    u32x4 xs[2], xa[4];
#pragma unroll
    for (int i = 0; i < 2; ++i) xs[i] = ld_nt(a.x + (2 * v + i) * DM + 8 * lane);
#pragma unroll
    for (int s = 0; s < 4; ++s) xa[s] = ld_nt(a.x + (c & 7) * DM + 128 * v + 32 * s + 8 * r);
    u32x4 wqr[4], w1r[2];
#pragma unroll
    for (int u = 0; u < 4; ++u)
      wqr[u] = ld_nt(a.wq + (size_t)(128 * v + 32 * u + (lane >> 1)) * 4096 + 16 * qb + 8 * (lane & 1));
#pragma unroll
    for (int u = 0; u < 2; ++u)
      w1r[u] = ld_nt(a.w1 + (size_t)(128 * v + 64 * u + lane) * FF + 8 * (h_on ? qb : 0));
    if (ROLE == 0) b1v = a.b1[8 * (h_on ? qb : 0) + (lane & 7)];
    __builtin_amdgcn_sched_barrier(0);
    if (lane == 0) __hip_atomic_fetch_add(flags + 3, 1u, __ATOMIC_RELAXED, __HIP_MEMORY_SCOPE_WORKGROUP);
    float* stat = reinterpret_cast<float*>(lds + L_XN);
#pragma unroll
    for (int i = 0; i < 2; ++i) {
      float s = 0.f;
#pragma unroll
      for (int k = 0; k < 4; ++k) s += bflo(xs[i][k]) + bfhi(xs[i][k]);
      s += dpp<XOR1>(s); s += dpp<XOR2>(s); s += dpp<XOR7>(s); s += dpp<XOR8>(s); s = xrow16_sum(s);
      const float mean = s * (1.0f / DM);
      float q2 = 0.f;
#pragma unroll
      for (int k = 0; k < 4; ++k) {
        const float d0 = bflo(xs[i][k]) - mean, d1 = bfhi(xs[i][k]) - mean;
        q2 = fmaf(d0, d0, q2); q2 = fmaf(d1, d1, q2);
      }
      q2 += dpp<XOR1>(q2); q2 += dpp<XOR2>(q2); q2 += dpp<XOR7>(q2); q2 += dpp<XOR8>(q2); q2 = xrow16_sum(q2);
      const float var_bf = bf16r(q2 * (1.0f / DM));
      if (lane == 0) { stat[4 * v + 2 * i] = bf16r(mean); stat[4 * v + 2 * i + 1] = bf16r(sqrtf(bf16r(var_bf + 1e-5f))); }
    }
    asm volatile("s_waitcnt lgkmcnt(0)" ::: "memory");
    if (lane == 0) __hip_atomic_fetch_add(flags + 4, 1u, __ATOMIC_RELAXED, __HIP_MEMORY_SCOPE_WORKGROUP);
    if (v == 1) STAMP(12);
    unsigned char* wqs = lds + L_WQS + v * 4352;
    unsigned char* w1s = lds + L_W1S + v * 2304;
#pragma unroll
    for (int u = 0; u < 4; ++u) {
      const int rr = 32 * u + (lane >> 1);
      *reinterpret_cast<u32x4*>(wqs + rr * 32 + (rr >> 3) * 16 + 16 * (lane & 1)) = wqr[u];
    }
#pragma unroll
    for (int u = 0; u < 2; ++u) {
      const int rr = 64 * u + lane;
      *reinterpret_cast<u32x4*>(w1s + rr * 16 + (rr >> 3) * 16) = w1r[u];
    }
    lds_wait_ge(flags + 4, 4u);
    const float mean_bf = stat[2 * (c & 7)], sd_bf = stat[2 * (c & 7) + 1];
    asm volatile("s_waitcnt lgkmcnt(0)" ::: "memory");
    f32x4 accq = {0.f, 0.f, 0.f, 0.f}, acch = {0.f, 0.f, 0.f, 0.f};
#pragma unroll
    for (int s = 0; s < 4; ++s) {
      u32x4 af;
#pragma unroll
      for (int k = 0; k < 4; ++k)
        af[k] = (c < 8) ? pack2(bf16r(bflo(xa[s][k]) - mean_bf) / sd_bf, bf16r(bfhi(xa[s][k]) - mean_bf) / sd_bf) : 0u;
      const s16x8 afr = __builtin_bit_cast(s16x8, af);
      s16x8 bq, bws;
#pragma unroll
      for (int jj = 0; jj < 8; ++jj) {
        const int k = 32 * s + 8 * r + jj;
        bq[jj] = *reinterpret_cast<const short*>(wqs + k * 32 + (k >> 3) * 16 + c * 2);
        bws[jj] = *reinterpret_cast<const short*>(w1s + k * 16 + (k >> 3) * 16 + (c & 7) * 2);
      }
      if (c >= 8) bws = s16x8{0, 0, 0, 0, 0, 0, 0, 0};
      accq = __builtin_amdgcn_mfma_f32_16x16x32_bf16(afr, bq, accq, 0, 0, 0);
      acch = __builtin_amdgcn_mfma_f32_16x16x32_bf16(afr, bws, acch, 0, 0, 0);
    }
    if (v == 1) STAMP(13);
    float* svc = reinterpret_cast<float*>(lds + L_SVC) + v * 256;
    if (lane < 32) {
#pragma unroll
      for (int k = 0; k < 4; ++k) {
        svc[(4 * r + k) * 16 + c] = accq[k];
        svc[128 + (4 * r + k) * 16 + c] = acch[k];
      }
    }
    if (ROLE == 1) {
      asm volatile("s_waitcnt lgkmcnt(0)" ::: "memory");
      if (lane == 0) __hip_atomic_fetch_add(flags + 1, 1u, __ATOMIC_RELAXED, __HIP_MEMORY_SCOPE_WORKGROUP);
    }
  }
  STAMP(24 + v);

  if (ROLE == 2) {
    const int t4 = tid - 256;
    lds_wait_ge(flags + 3, 4u);
    u32x4 st[6];
#pragma unroll
    for (int u = 0; u < 4; ++u) {
      const int e = t4 + 256 * u, row = e >> 3, seg = e & 7;
      st[u] = ld_nt(a.wo + (size_t)(128 * hi + row) * DM + 64 * cj + 8 * seg);
    }
#pragma unroll
    for (int u = 0; u < 2; ++u) {
      const int e = t4 + 256 * u, row = e >> 3, seg = e & 7;
      st[4 + u] = ld_nt(a.w2 + (size_t)(56 * hi + (row < 56 ? row : 0)) * DM + 64 * cj + 8 * seg);
    }
#pragma unroll
    for (int i = 0; i < 4 * NG; ++i) kv[i] = ld_nt(a.kc + kvbase + (size_t)i * 4 * DH);
    __builtin_amdgcn_sched_barrier(0);
#pragma unroll
    for (int u = 0; u < 4; ++u) {
      const int e = t4 + 256 * u, row = e >> 3, seg = e & 7;
      *reinterpret_cast<u32x4*>(lds + L_WO + row * WROW + 16 * seg) = st[u];
    }
#pragma unroll
    for (int u = 0; u < 2; ++u) {
      const int e = t4 + 256 * u, row = e >> 3, seg = e & 7;
      *reinterpret_cast<u32x4*>(lds + L_W2 + row * WROW + 16 * seg) = row < 56 ? st[4 + u] : u32x4{0u, 0u, 0u, 0u};
    }
    reinterpret_cast<u32*>(lds + L_H)[256 + t4] = 0u;
    reinterpret_cast<u32*>(lds + L_O)[512 + t4] = 0u;
    reinterpret_cast<u32*>(lds + L_O)[768 + t4] = 0u;
    asm volatile("s_waitcnt lgkmcnt(0)" ::: "memory");
    if (lane == 0) __hip_atomic_fetch_add(flags + 2, 1u, __ATOMIC_RELAXED, __HIP_MEMORY_SCOPE_WORKGROUP);
  }
  if (ROLE == 1) {
#pragma unroll
    for (int i = 0; i < 4 * NG; ++i) kv[i] = ld_nt(a.kc + kvbase + (size_t)i * 4 * DH);
    __builtin_amdgcn_sched_barrier(0);
  }

  if (ROLE == 0) {
    lds_wait_ge(flags + 1, 3u);
    const float* svc = reinterpret_cast<const float*>(lds + L_SVC);
    if (lane < 32) {
      const int b = lane >> 2, cq = lane & 3;
      float sv[4];
#pragma unroll
      for (int e = 0; e < 4; ++e) {
        float s0 = 0.f;
#pragma unroll
        for (int u = 0; u < 4; ++u) s0 += svc[u * 256 + b * 16 + 4 * cq + e];
        sv[e] = s0;
      }
      put_granule(rs, G_Q + b * 1024 + 4 * qb + cq, pack2(sv[0], sv[1]), pack2(sv[2], sv[3]), tag);
    }
    {
      const int b = lane >> 3, col = lane & 7;
      float s0 = 0.f;
#pragma unroll
      for (int u = 0; u < 4; ++u) s0 += svc[u * 256 + 128 + b * 16 + col];
      const float t = bf16r(bf16r(s0) + bfu((u16)b1v));
      const float inner = 0.7978845608028654f * (t + 0.044715f * t * t * t);
      const float hv = 0.5f * t * (1.0f + tanhf(inner));
      const float h1 = dpp<XOR1>(hv);
      const u32 pr = (lane & 1) ? pack2(h1, hv) : pack2(hv, h1);
      const u32 pr2 = __builtin_bit_cast(u32, dpp<XOR2>(__builtin_bit_cast(float, pr)));
      if (h_on && (lane & 3) == 0) put_granule(rs, G_H + b * 448 + 2 * qb + (col >> 2), pr, pr2, tag);
    }
    STAMP(1);
    u32x4 gq[2], gh[2];
    for (u32 spins = 0; spins < SPIN_CAP; ++spins) {
      asm volatile("" ::: "memory");
      bool ok = true;
#pragma unroll
      for (int k = 0; k < 2; ++k) { gq[k] = get_granule(rs, G_Q + ab * 1024 + ahkv * 128 + 64 * k + lane); ok &= tag_ok(gq[k], tag); }
      if (__all(ok)) break;
      __builtin_amdgcn_s_sleep(1);
    }
    u64* qs = reinterpret_cast<u64*>(lds + L_Q);
#pragma unroll
    for (int k = 0; k < 2; ++k) qs[64 * k + lane] = ((u64)gq[k][1] << 32) | gq[k][0];
    asm volatile("s_waitcnt lgkmcnt(0)" ::: "memory");
    if (lane == 0) __hip_atomic_store(flags, 1u, __ATOMIC_RELAXED, __HIP_MEMORY_SCOPE_WORKGROUP);
    STAMP(2);
#pragma unroll
    for (int i = 0; i < 4 * NG; ++i) kv[i] = ld_nt(a.kc + kvbase + (size_t)i * 4 * DH);
    u32 hg[2]; int hidx[2];
#pragma unroll
    for (int k = 0; k < 2; ++k) {
      const int idx = min(lane + 64 * k, 111);
      const int b = idx / 14, t = idx % 14;
      hg[k] = G_H + b * 448 + 14 * hi + t;
      hidx[k] = b * 16 + t;
    }
    for (u32 spins = 0; spins < SPIN_CAP; ++spins) {
      asm volatile("" ::: "memory");
      bool ok = true;
#pragma unroll
      for (int k = 0; k < 2; ++k) { gh[k] = get_granule(rs, hg[k]); ok &= tag_ok(gh[k], tag); }
      if (__all(ok)) break;
      __builtin_amdgcn_s_sleep(1);
    }
    u64* hs = reinterpret_cast<u64*>(lds + L_H);
#pragma unroll
    for (int k = 0; k < 2; ++k) if (lane + 64 * k < 112) hs[hidx[k]] = ((u64)gh[k][1] << 32) | gh[k][0];
    if (lane < 16) hs[(lane >> 1) * 16 + 14 + (lane & 1)] = 0ull;
    lds_wait_ge(flags + 2, 4u);
    asm volatile("s_waitcnt lgkmcnt(0)" ::: "memory");
#pragma unroll
    for (int ct = 0; ct < 4; ++ct) {
      f32x4 acc = {0.f, 0.f, 0.f, 0.f};
#pragma unroll
      for (int s = 0; s < 2; ++s) {
        const u32x4 af = *reinterpret_cast<const u32x4*>(lds + L_H + c * 128 + (32 * s + 8 * r) * 2);
        s16x8 bf;
#pragma unroll
        for (int jj = 0; jj < 8; ++jj)
          bf[jj] = *reinterpret_cast<const short*>(lds + L_W2 + (32 * s + 8 * r + jj) * WROW + (16 * ct + c) * 2);
        acc = __builtin_amdgcn_mfma_f32_16x16x32_bf16(__builtin_bit_cast(s16x8, af), bf, acc, 0, 0, 0);
      }
#pragma unroll
      for (int k = 0; k < 4; ++k) {
        const float nb = dpp<XOR1>(acc[k]);
        if (lane < 32 && (c & 1) == 0)
          put_granule(rs, G_Y + ((cj * NB + (4 * r + k)) * 32 + hi) * 32 + 8 * ct + (c >> 1), __float_as_uint(acc[k]), __float_as_uint(nb), tag);
      }
    }
    STAMP(3);
  } else {
    lds_wait_ge(flags, 1u);
  }

  u32x4 qpk[4];
#pragma unroll
  for (int g = 0; g < 4; ++g) qpk[g] = *reinterpret_cast<const u32x4*>(lds + L_Q + g * 256 + c * 16);
  float sc[NG];
#pragma unroll
  for (int grp = 0; grp < NG; ++grp) {
    float x[16];
#pragma unroll
    for (int t = 0; t < 4; ++t)
#pragma unroll
      for (int g = 0; g < 4; ++g) {
        float d = 0.f;
#pragma unroll
        for (int k = 0; k < 4; ++k) d = dot2(kv[4 * grp + t][k], qpk[g][k], d);
        x[t * 4 + g] = d;
      }
#define TR_STEP(HALF, CTRL, BIT) _Pragma("unroll") for (int h = 0; h < (HALF); ++h) { \
      const float lo_ = x[h], hi_ = x[h + (HALF)]; const float t1_ = lo_ + dpp<CTRL>(lo_), t2_ = hi_ + dpp<CTRL>(hi_); \
      x[h] = (c & (BIT)) ? t2_ : t1_; }
    TR_STEP(8, XOR8, 8) TR_STEP(4, XOR7, 4) TR_STEP(2, XOR2, 2) TR_STEP(1, XOR1, 1)
#undef TR_STEP
    sc[grp] = bf16r(bf16r(x[0]) * 0.08838834764831845f);
#pragma unroll
    for (int t = 0; t < 4; ++t) kv[4 * grp + t] = ld_nt(a.vc + kvbase + (size_t)(4 * grp + t) * 4 * DH);
    __builtin_amdgcn_sched_barrier(0);
  }
  STAMP(48 + v);
  float m = sc[0];
#pragma unroll
  for (int grp = 1; grp < NG; ++grp) m = fmaxf(m, sc[grp]);
  m = fmaxf(m, dpp<ROR4>(m)); m = fmaxf(m, dpp<XOR8>(m)); m = xrow16_max(m);
  float l = 0.f;
  {
    float* pl = reinterpret_cast<float*>(lds + L_P) + v * 576;
#pragma unroll
    for (int grp = 0; grp < NG; ++grp) {
      const float p = __builtin_amdgcn_exp2f((sc[grp] - m) * LOG2E);
      l += p;
      pl[(16 * grp + 4 * (c >> 2) + r) * 4 + (c & 3)] = p;
    }
  }
  l += dpp<ROR4>(l); l += dpp<XOR8>(l); l = xrow16_sum(l);
  asm volatile("s_waitcnt lgkmcnt(0)" ::: "memory");

  float acc[32];
#pragma unroll
  for (int k = 0; k < 32; ++k) acc[k] = 0.f;
  {
    const f32x4* pl4 = reinterpret_cast<const f32x4*>(lds + L_P) + v * 144;
#pragma unroll
    for (int i = 0; i < 4 * NG; ++i) {
      const f32x4 p = pl4[4 * i + r];
      float vf[8];
#pragma unroll
      for (int k = 0; k < 4; ++k) { vf[2 * k] = bflo(kv[i][k]); vf[2 * k + 1] = bfhi(kv[i][k]); }
#pragma unroll
      for (int g = 0; g < 4; ++g)
#pragma unroll
        for (int k = 0; k < 8; ++k) acc[g * 8 + k] = fmaf(p[g], vf[k], acc[g * 8 + k]);
    }
  }
#pragma unroll
  for (int k = 0; k < 32; ++k) acc[k] = xrow16_sum(acc[k]);
  STAMP(40 + v);
  {
    float* mrg = reinterpret_cast<float*>(lds + L_MRG) + v * 4 * 132;
    if (r == 0) {
#pragma unroll
      for (int g = 0; g < 4; ++g) {
        *reinterpret_cast<f32x4*>(mrg + g * 132 + 4 + 8 * c) = f32x4{acc[g * 8], acc[g * 8 + 1], acc[g * 8 + 2], acc[g * 8 + 3]};
        *reinterpret_cast<f32x4*>(mrg + g * 132 + 4 + 8 * c + 4) = f32x4{acc[g * 8 + 4], acc[g * 8 + 5], acc[g * 8 + 6], acc[g * 8 + 7]};
      }
      if (c < 4) { mrg[c * 132] = m; mrg[c * 132 + 1] = l; }
    }
  }
  if (ROLE == 0 && hi < 8) {
    const u32 yg = G_Y + (cj * NB + hi) * 32 * 32 + (lane & 31) + (lane >> 5) * 32;
    float y0 = 0.f, y1 = 0.f;
    for (u32 spins = 0; spins < SPIN_CAP; ++spins) {
      asm volatile("" ::: "memory");
      bool ok = true; y0 = 0.f; y1 = 0.f;
#pragma unroll
      for (int i = 0; i < 16; ++i) { const u32x4 g = get_granule(rs, yg + 64 * i); y0 += __uint_as_float(g[0]); y1 += __uint_as_float(g[1]); ok &= tag_ok(g, tag); }
      if (__all(ok)) break;
      __builtin_amdgcn_s_sleep(8);
    }
    {
      auto t0 = __builtin_amdgcn_permlane32_swap(__float_as_uint(y0), __float_as_uint(y0), false, false);
      y0 = __uint_as_float(t0[0]) + __uint_as_float(t0[1]);
      auto t1 = __builtin_amdgcn_permlane32_swap(__float_as_uint(y1), __float_as_uint(y1), false, false);
      y1 = __uint_as_float(t1[0]) + __uint_as_float(t1[1]);
    }
    if (lane < 32) {
      const u32 b2p = *reinterpret_cast<const u32*>(a.b2 + 64 * cj + 2 * lane);
      const float ya = bf16r(bf16r(y0) + bflo(b2p)), yb = bf16r(bf16r(y1) + bfhi(b2p));
      reinterpret_cast<float*>(lds + L_G)[2 * lane] = bf16r(ya / (1.0f + __expf(-ya)));
      reinterpret_cast<float*>(lds + L_G)[2 * lane + 1] = bf16r(yb / (1.0f + __expf(-yb)));
    }
  }
  wg_barrier();
  if (v == 0) STAMP(7);

  if (tid < 256) {
    const int g = tid >> 6, d2 = tid & 63;
    const float* mrg = reinterpret_cast<const float*>(lds + L_MRG) + g * 132;
    float M = mrg[0];
#pragma unroll
    for (int u = 1; u < 8; ++u) M = fmaxf(M, mrg[u * 4 * 132]);
    float L = 0.f, o0 = 0.f, o1 = 0.f;
#pragma unroll
    for (int u = 0; u < 8; ++u) {
      const float wt = __builtin_amdgcn_exp2f((mrg[u * 4 * 132] - M) * LOG2E);
      L = fmaf(wt, mrg[u * 4 * 132 + 1], L);
      o0 = fmaf(wt, mrg[u * 4 * 132 + 4 + 2 * d2], o0);
      o1 = fmaf(wt, mrg[u * 4 * 132 + 5 + 2 * d2], o1);
    }
    const u32 pg = G_P + ((ab * HQ + ahkv * 4 + g) * 4 + split) * 66;
    put_granule(rs, pg + d2, __float_as_uint(o0), __float_as_uint(o1), tag);
    if (d2 == 0) put_granule(rs, pg + 64, __float_as_uint(M), __float_as_uint(L), tag);
  }

  {
    const u32 pg = G_P + (v * HQ + hi) * 4 * 66;
    probe_wait(rs, pg + (lane & 3) * 66 + 64, lane < 4, tag);
    u32x4 go[4], gm[4];
    for (u32 spins = 0; spins < SPIN_CAP; ++spins) {
      asm volatile("" ::: "memory");
      bool ok = true;
#pragma unroll
      for (int s = 0; s < 4; ++s) {
        go[s] = get_granule(rs, pg + s * 66 + lane);
        gm[s] = get_granule(rs, pg + s * 66 + 64);
        ok &= tag_ok(go[s], tag) & tag_ok(gm[s], tag);
      }
      if (__all(ok)) break;
      __builtin_amdgcn_s_sleep(2);
    }
    float M = __uint_as_float(gm[0][0]);
#pragma unroll
    for (int s = 1; s < 4; ++s) M = fmaxf(M, __uint_as_float(gm[s][0]));
    float L = 0.f, a0 = 0.f, a1 = 0.f;
#pragma unroll
    for (int s = 0; s < 4; ++s) {
      const float wt = __builtin_amdgcn_exp2f((__uint_as_float(gm[s][0]) - M) * LOG2E);
      L = fmaf(wt, __uint_as_float(gm[s][1]), L);
      a0 = fmaf(wt, __uint_as_float(go[s][0]), a0);
      a1 = fmaf(wt, __uint_as_float(go[s][1]), a1);
    }
    const float inv = 1.0f / L;
    reinterpret_cast<u32*>(lds + L_O)[v * 64 + lane] = pack2(a0 * inv, a1 * inv);
  }
  wg_barrier();
  if (v == 0) STAMP(8);
  if (ROLE <= 1) {
    const int ct = v;
    f32x4 acc4 = {0.f, 0.f, 0.f, 0.f};
#pragma unroll
    for (int s = 0; s < 4; ++s) {
      const u32x4 af = *reinterpret_cast<const u32x4*>(lds + L_O + c * 256 + (32 * s + 8 * r) * 2);
      s16x8 bf;
#pragma unroll
      for (int jj = 0; jj < 8; ++jj)
        bf[jj] = *reinterpret_cast<const short*>(lds + L_WO + (32 * s + 8 * r + jj) * WROW + (16 * ct + c) * 2);
      acc4 = __builtin_amdgcn_mfma_f32_16x16x32_bf16(__builtin_bit_cast(s16x8, af), bf, acc4, 0, 0, 0);
    }
#pragma unroll
    for (int k = 0; k < 4; ++k) {
      const float nb = dpp<XOR1>(acc4[k]);
      if (lane < 32 && (c & 1) == 0)
        put_granule(rs, G_S + ((cj * NB + (4 * r + k)) * 32 + hi) * 32 + 8 * ct + (c >> 1), __float_as_uint(acc4[k]), __float_as_uint(nb), tag);
    }
  }
  if (ROLE == 2 && hi < 8 && v == 4) {
    const u32 sg = G_S + (cj * NB + hi) * 32 * 32;
    probe_wait(rs, sg + (lane >> 1) * 32 + 31 - (lane & 1) * 16, true, tag);
    float s0 = 0.f, s1 = 0.f;
    for (u32 spins = 0; spins < SPIN_CAP; ++spins) {
      asm volatile("" ::: "memory");
      bool ok = true; s0 = 0.f; s1 = 0.f;
#pragma unroll
      for (int i = 0; i < 16; ++i) { const u32x4 g = get_granule(rs, sg + lane + 64 * i); s0 += __uint_as_float(g[0]); s1 += __uint_as_float(g[1]); ok &= tag_ok(g, tag); }
      if (__all(ok)) break;
      __builtin_amdgcn_s_sleep(2);
    }
    {
      auto t0 = __builtin_amdgcn_permlane32_swap(__float_as_uint(s0), __float_as_uint(s0), false, false);
      s0 = __uint_as_float(t0[0]) + __uint_as_float(t0[1]);
      auto t1 = __builtin_amdgcn_permlane32_swap(__float_as_uint(s1), __float_as_uint(s1), false, false);
      s1 = __uint_as_float(t1[0]) + __uint_as_float(t1[1]);
    }
    if (lane < 32) {
      const float g0 = reinterpret_cast<const float*>(lds + L_G)[2 * lane], g1 = reinterpret_cast<const float*>(lds + L_G)[2 * lane + 1];
      const u32 xp = *reinterpret_cast<const u32*>(a.x + hi * DM + 64 * cj + 2 * lane);
      const float r0 = bflo(xp) + bf16r(bf16r(s0) * g0), r1 = bfhi(xp) + bf16r(bf16r(s1) * g1);
      *reinterpret_cast<u32*>(a.out + hi * DM + 64 * cj + 2 * lane) = pack2(r0, r1);
    }
    if (w == 0 && lane == 0)
      __hip_atomic_store(a.ws, ((u64)tag.hi << 32) | tag.lo, __ATOMIC_RELAXED, __HIP_MEMORY_SCOPE_AGENT);
    STAMP(9);
  }
}

__global__ void __launch_bounds__(NTHR, 2) fused_decode_kernel(Params a) {
  __shared__ __attribute__((aligned(16))) unsigned char lds[L_END];
  const int v = __builtin_amdgcn_readfirstlane(threadIdx.x >> 6);
  if (v == 0) body<0>(a, lds, 0);
  else if (v < 4) body<1>(a, lds, v);
  else body<2>(a, lds, v);
}

extern "C" void kernel_launch(void* const* d_in, const int* in_sizes, int n_in,
                              void* d_out, int out_size, void* d_ws, size_t ws_size,
                              hipStream_t stream) {
  Params p;
  p.x = (const u16*)d_in[0];
  p.kc = (const u16*)d_in[1];
  p.vc = (const u16*)d_in[2];
  p.wq = (const u16*)d_in[3];
  p.wo = (const u16*)d_in[4];
  p.w1 = (const u16*)d_in[6];
  p.b1 = (const u16*)d_in[7];
  p.w2 = (const u16*)d_in[8];
  p.b2 = (const u16*)d_in[9];
  p.out = (u16*)d_out;
  p.ws = (u64*)d_ws;
  hipLaunchKernelGGL(fused_decode_kernel, dim3(NWG), dim3(NTHR), 0, stream, p);
}
```

```cpp
#include <hip/hip_runtime.h>
#include <hip/hip_bf16.h>
#include <cstdint>

typedef unsigned int u32;
typedef unsigned long long u64;
typedef unsigned short u16;
using u32x4 = __attribute__((ext_vector_type(4))) u32;
using f32x4 = __attribute__((ext_vector_type(4))) float;
using f32x2 = __attribute__((ext_vector_type(2))) float;
using s16x8 = __attribute__((ext_vector_type(8))) short;
using bf16x2 = __attribute__((ext_vector_type(2))) __bf16;

constexpr int NB = 8, DM = 512, HQ = 32, HKV = 8, SK = 4096, DH = 128, FF = 1792;
constexpr int NWG = 256, NTHR = 512;
constexpr u32 SPIN_CAP = 1u << 20;
constexpr float LOG2E = 1.4426950408889634f;

constexpr u32 G_Q = 16;
constexpr u32 G_H = G_Q + NB * 1024;
constexpr u32 G_P = G_H + NB * 448;
constexpr u32 G_S = G_P + NB * HQ * 4 * 66;
constexpr u32 G_Y = G_S + 8 * NB * 32 * 32;
constexpr u32 G_END = G_Y + 8 * NB * 32 * 32;

__device__ __forceinline__ float bf16r(float x) { return (float)(__bf16)x; }
__device__ __forceinline__ u32 pack2(float lo, float hi) {
  bf16x2 v = {(__bf16)lo, (__bf16)hi};
  return __builtin_bit_cast(u32, v);
}
__device__ __forceinline__ float bflo(u32 x) { return __builtin_bit_cast(float, x << 16); }
__device__ __forceinline__ float bfhi(u32 x) { return __builtin_bit_cast(float, x & 0xffff0000u); }
__device__ __forceinline__ float bfu(u16 x) { return __builtin_bit_cast(float, (u32)x << 16); }
__device__ __forceinline__ float dot2(u32 k, u32 q, float acc) {
  return __builtin_amdgcn_fdot2_f32_bf16(__builtin_bit_cast(bf16x2, k), __builtin_bit_cast(bf16x2, q), acc, false);
}
template <int CTRL> __device__ __forceinline__ float dpp(float x) {
  return __builtin_bit_cast(float, __builtin_amdgcn_mov_dpp(__builtin_bit_cast(int, x), CTRL, 0xf, 0xf, true));
}
constexpr int XOR1 = 0xB1, XOR2 = 0x4E, XOR7 = 0x141, XOR8 = 0x128, ROR4 = 0x124;
__device__ __forceinline__ float xrow16_max(float x) {
  auto s = __builtin_amdgcn_permlane16_swap(__float_as_uint(x), __float_as_uint(x), false, false);
  x = fmaxf(__uint_as_float(s[0]), __uint_as_float(s[1]));
  auto t = __builtin_amdgcn_permlane32_swap(__float_as_uint(x), __float_as_uint(x), false, false);
  return fmaxf(__uint_as_float(t[0]), __uint_as_float(t[1]));
}
__device__ __forceinline__ float xrow16_sum(float x) {
  auto s = __builtin_amdgcn_permlane16_swap(__float_as_uint(x), __float_as_uint(x), false, false);
  x = __uint_as_float(s[0]) + __uint_as_float(s[1]);
  auto t = __builtin_amdgcn_permlane32_swap(__float_as_uint(x), __float_as_uint(x), false, false);
  return __uint_as_float(t[0]) + __uint_as_float(t[1]);
}
typedef __amdgpu_buffer_rsrc_t rsrc_t;
struct Tag { u32 lo, hi; };
__device__ __forceinline__ void put_granule(rsrc_t rs, u32 g, u32 v0, u32 v1, Tag t) {
  __builtin_amdgcn_raw_buffer_store_b128(u32x4{v0, v1, t.lo, t.hi}, rs, (int)(g * 16u), 0, 16);
}
__device__ __forceinline__ u32x4 get_granule(rsrc_t rs, u32 g) {
  return __builtin_amdgcn_raw_buffer_load_b128(rs, (int)(g * 16u), 0, 16);
}
__device__ __forceinline__ bool tag_ok(u32x4 g, Tag t) { return (g[2] == t.lo) & (g[3] == t.hi); }
__device__ __forceinline__ void wg_barrier() {
  asm volatile("s_waitcnt lgkmcnt(0)" ::: "memory");
  __builtin_amdgcn_s_barrier();
  asm volatile("" ::: "memory");
}
__device__ __forceinline__ u32x4 ld_nt(const u16* p) {
  return __builtin_nontemporal_load(reinterpret_cast<const u32x4*>(p));
}
__device__ __forceinline__ u32x4 ld_pl(const u16* p) { return *reinterpret_cast<const u32x4*>(p); }
#define LD_SVC(p) ld_nt(p)
#define LD_STG(p) ld_nt(p)
__device__ __forceinline__ u32 lds_flag_load(u32* f) {
  return (u32)__builtin_amdgcn_readfirstlane(__hip_atomic_load(f, __ATOMIC_RELAXED, __HIP_MEMORY_SCOPE_WORKGROUP));
}
__device__ __forceinline__ void lds_wait_ge(u32* f, u32 want) {
  for (u32 spins = 0; spins < SPIN_CAP * 8u; ++spins) {
    if (lds_flag_load(f) >= want) break;
    __builtin_amdgcn_s_sleep(1);
  }
  asm volatile("" ::: "memory");
}
__device__ __forceinline__ void probe_wait(rsrc_t rs, u32 g, bool active, Tag t) {
  for (u32 spins = 0; spins < SPIN_CAP; ++spins) {
    asm volatile("" ::: "memory");
    bool ok = true;
    if (active) ok = tag_ok(get_granule(rs, g), t);
    if (__all(ok)) break;
    __builtin_amdgcn_s_sleep(2);
  }
}

#define STAMP(k) do { } while (0)

struct Params {
  const u16* x; const u16* kc; const u16* vc; const u16* wq; const u16* wo;
  const u16* w1; const u16* b1; const u16* w2; const u16* b2;
  u16* out; u64* ws;
};

constexpr int WROW = 144;
constexpr int L_WO = 0;
constexpr int L_W2 = L_WO + 128 * WROW;
constexpr int L_SVC = L_W2 + 64 * WROW;
constexpr int L_Q = L_SVC + 4 * 2 * 8 * 16 * 4;
constexpr int L_H = L_Q + 1024;
constexpr int L_O = L_H + 16 * 32 * 4;
constexpr int L_G = L_O + 16 * 64 * 4;
constexpr int L_FLAG = L_G + 256;
constexpr int L_P = L_FLAG + 32;
constexpr int L_MRG = L_P + 8 * 144 * 4 * 4;
constexpr int L_WQS = L_MRG + 8 * 4 * 132 * 4;
constexpr int L_W1S = L_WQS + 4 * 4352;
constexpr int L_XN = L_W1S + 4 * 2304;
constexpr int L_END = L_XN + 64;

template <int ROLE>
__device__ __forceinline__ void body(const Params& a, unsigned char* lds, const int v) {
  constexpr int NG = (ROLE == 0) ? 1 : 9;
  const int w = blockIdx.x;
  const int tid = threadIdx.x, lane = tid & 63;
  const int r = lane >> 4, c = lane & 15;
  const int bh = w >> 2, split = w & 3;
  const int ab = bh >> 3, ahkv = bh & 7;
  const int hi = w >> 3, cj = w & 7;
  const int qb = (w & 7) * 32 + (w >> 3);
  const bool h_on = (qb < 224);
  const rsrc_t rs = __builtin_amdgcn_make_buffer_rsrc((void*)a.ws, 0, (int)(G_END * 16u), 0x00020000);
  Tag tag;
  {
    const u64 e = __hip_atomic_load(a.ws, __ATOMIC_RELAXED, __HIP_MEMORY_SCOPE_AGENT) + 1ull;
    tag.lo = (u32)__builtin_amdgcn_readfirstlane((u32)e);
    tag.hi = (u32)__builtin_amdgcn_readfirstlane((u32)(e >> 32));
  }
  u32* flags = reinterpret_cast<u32*>(lds + L_FLAG);
  STAMP(16 + v);

  if (ROLE == 0) {
    if (lane < 8) flags[lane] = 0u;
  }
  wg_barrier();
  __builtin_amdgcn_s_setprio(3);

  const int key0 = (ROLE == 0) ? 0 : 16 + 144 * (v - 1);
  const size_t kvbase = ((size_t)bh * SK + split * 1024 + key0) * DH + (size_t)r * DH + 8 * c;
  const size_t kfbase = ((size_t)bh * SK + split * 1024 + key0) * DH + (size_t)c * DH + 8 * r;
#define VPRE_G 3
  constexpr int VPG = (ROLE == 0) ? 0 : VPRE_G;
  u32x4 kv[4 * NG];
  u32x4 vpre[4 * VPG + 1];
  u32 b1v = 0;

  if (ROLE <= 1) {
    u32x4 xs[2], xa[4];
#pragma unroll
    for (int i = 0; i < 2; ++i) xs[i] = ld_nt(a.x + (2 * v + i) * DM + 8 * lane);
#pragma unroll
    for (int s = 0; s < 4; ++s) xa[s] = ld_nt(a.x + (c & 7) * DM + 128 * v + 32 * s + 8 * r);
    u32x4 wqr[4], w1r[2];
#pragma unroll
    for (int u = 0; u < 4; ++u)
      wqr[u] = LD_SVC(a.wq + (size_t)(128 * v + 32 * u + (lane >> 1)) * 4096 + 16 * qb + 8 * (lane & 1));
#pragma unroll
    for (int u = 0; u < 2; ++u)
      w1r[u] = LD_SVC(a.w1 + (size_t)(128 * v + 64 * u + lane) * FF + 8 * (h_on ? qb : 0));
    if (ROLE == 0) b1v = a.b1[8 * (h_on ? qb : 0) + (lane & 7)];
    __builtin_amdgcn_sched_barrier(0);
    if (lane == 0) __hip_atomic_fetch_add(flags + 3, 1u, __ATOMIC_RELAXED, __HIP_MEMORY_SCOPE_WORKGROUP);
    float* stat = reinterpret_cast<float*>(lds + L_XN);
#pragma unroll
    for (int i = 0; i < 2; ++i) {
      float s = 0.f;
#pragma unroll
      for (int k = 0; k < 4; ++k) s += bflo(xs[i][k]) + bfhi(xs[i][k]);
      s += dpp<XOR1>(s); s += dpp<XOR2>(s); s += dpp<XOR7>(s); s += dpp<XOR8>(s); s = xrow16_sum(s);
      const float mean = s * (1.0f / DM);
      float q2 = 0.f;
#pragma unroll
      for (int k = 0; k < 4; ++k) {
        const float d0 = bflo(xs[i][k]) - mean, d1 = bfhi(xs[i][k]) - mean;
        q2 = fmaf(d0, d0, q2); q2 = fmaf(d1, d1, q2);
      }
      q2 += dpp<XOR1>(q2); q2 += dpp<XOR2>(q2); q2 += dpp<XOR7>(q2); q2 += dpp<XOR8>(q2); q2 = xrow16_sum(q2);
      const float var_bf = bf16r(q2 * (1.0f / DM));
      if (lane == 0) { stat[4 * v + 2 * i] = bf16r(mean); stat[4 * v + 2 * i + 1] = bf16r(sqrtf(bf16r(var_bf + 1e-5f))); }
    }
    asm volatile("s_waitcnt lgkmcnt(0)" ::: "memory");
    if (lane == 0) __hip_atomic_fetch_add(flags + 4, 1u, __ATOMIC_RELAXED, __HIP_MEMORY_SCOPE_WORKGROUP);
    if (v == 1) STAMP(12);
    unsigned char* wqs = lds + L_WQS + v * 4352;
    unsigned char* w1s = lds + L_W1S + v * 2304;
#pragma unroll
    for (int u = 0; u < 4; ++u) {
      const int rr = 32 * u + (lane >> 1);
      *reinterpret_cast<u32x4*>(wqs + rr * 32 + (rr >> 3) * 16 + 16 * (lane & 1)) = wqr[u];
    }
#pragma unroll
    for (int u = 0; u < 2; ++u) {
      const int rr = 64 * u + lane;
      *reinterpret_cast<u32x4*>(w1s + rr * 16 + (rr >> 3) * 16) = w1r[u];
    }
    lds_wait_ge(flags + 4, 4u);
    const float mean_bf = stat[2 * (c & 7)], sd_bf = stat[2 * (c & 7) + 1];
    asm volatile("s_waitcnt lgkmcnt(0)" ::: "memory");
    f32x4 accq = {0.f, 0.f, 0.f, 0.f}, acch = {0.f, 0.f, 0.f, 0.f};
#pragma unroll
    for (int s = 0; s < 4; ++s) {
      u32x4 af;
#pragma unroll
      for (int k = 0; k < 4; ++k)
        af[k] = (c < 8) ? pack2(bf16r(bflo(xa[s][k]) - mean_bf) / sd_bf, bf16r(bfhi(xa[s][k]) - mean_bf) / sd_bf) : 0u;
      const s16x8 afr = __builtin_bit_cast(s16x8, af);
      s16x8 bq, bws;
#pragma unroll
      for (int jj = 0; jj < 8; ++jj) {
        const int k = 32 * s + 8 * r + jj;
        bq[jj] = *reinterpret_cast<const short*>(wqs + k * 32 + (k >> 3) * 16 + c * 2);
        bws[jj] = *reinterpret_cast<const short*>(w1s + k * 16 + (k >> 3) * 16 + (c & 7) * 2);
      }
      if (c >= 8) bws = s16x8{0, 0, 0, 0, 0, 0, 0, 0};
      accq = __builtin_amdgcn_mfma_f32_16x16x32_bf16(afr, bq, accq, 0, 0, 0);
      acch = __builtin_amdgcn_mfma_f32_16x16x32_bf16(afr, bws, acch, 0, 0, 0);
    }
    if (v == 1) STAMP(13);
    float* svc = reinterpret_cast<float*>(lds + L_SVC) + v * 256;
    if (lane < 32) {
#pragma unroll
      for (int k = 0; k < 4; ++k) {
        svc[(4 * r + k) * 16 + c] = accq[k];
        svc[128 + (4 * r + k) * 16 + c] = acch[k];
      }
    }
    if (ROLE == 1) {
      asm volatile("s_waitcnt lgkmcnt(0)" ::: "memory");
      if (lane == 0) __hip_atomic_fetch_add(flags + 1, 1u, __ATOMIC_RELAXED, __HIP_MEMORY_SCOPE_WORKGROUP);
    }
  }
  STAMP(24 + v);

  if (ROLE == 2) {
    const int t4 = tid - 256;
    lds_wait_ge(flags + 3, 4u);
    u32x4 st[6];
#pragma unroll
    for (int u = 0; u < 4; ++u) {
      const int e = t4 + 256 * u, row = e >> 3, seg = e & 7;
      st[u] = LD_STG(a.wo + (size_t)(128 * hi + row) * DM + 64 * cj + 8 * seg);
    }
#pragma unroll
    for (int u = 0; u < 2; ++u) {
      const int e = t4 + 256 * u, row = e >> 3, seg = e & 7;
      st[4 + u] = LD_STG(a.w2 + (size_t)(56 * hi + (row < 56 ? row : 0)) * DM + 64 * cj + 8 * seg);
    }
#pragma unroll
    for (int i = 0; i < 4 * NG; ++i) kv[i] = ld_nt(a.kc + kfbase + (size_t)(i >> 2) * 16 * DH + (i & 3) * 32);
#pragma unroll
    for (int i = 0; i < 4 * VPG; ++i) vpre[i] = ld_nt(a.vc + kvbase + (size_t)i * 4 * DH);
    __builtin_amdgcn_sched_barrier(0);
#pragma unroll
    for (int u = 0; u < 4; ++u) {
      const int e = t4 + 256 * u, row = e >> 3, seg = e & 7;
      *reinterpret_cast<u32x4*>(lds + L_WO + row * WROW + 16 * seg) = st[u];
    }
#pragma unroll
    for (int u = 0; u < 2; ++u) {
      const int e = t4 + 256 * u, row = e >> 3, seg = e & 7;
      *reinterpret_cast<u32x4*>(lds + L_W2 + row * WROW + 16 * seg) = row < 56 ? st[4 + u] : u32x4{0u, 0u, 0u, 0u};
    }
    reinterpret_cast<u32*>(lds + L_H)[256 + t4] = 0u;
    reinterpret_cast<u32*>(lds + L_O)[512 + t4] = 0u;
    reinterpret_cast<u32*>(lds + L_O)[768 + t4] = 0u;
    asm volatile("s_waitcnt lgkmcnt(0)" ::: "memory");
    if (lane == 0) __hip_atomic_fetch_add(flags + 2, 1u, __ATOMIC_RELAXED, __HIP_MEMORY_SCOPE_WORKGROUP);
  }
  if (ROLE == 1) {
#pragma unroll
    for (int i = 0; i < 4 * NG; ++i) kv[i] = ld_nt(a.kc + kfbase + (size_t)(i >> 2) * 16 * DH + (i & 3) * 32);
#pragma unroll
    for (int i = 0; i < 4 * VPG; ++i) vpre[i] = ld_nt(a.vc + kvbase + (size_t)i * 4 * DH);
    __builtin_amdgcn_sched_barrier(0);
  }

  if (ROLE == 0) {
    lds_wait_ge(flags + 1, 3u);
    const float* svc = reinterpret_cast<const float*>(lds + L_SVC);
    if (lane < 32) {
      const int b = lane >> 2, cq = lane & 3;
      float sv[4];
#pragma unroll
      for (int e = 0; e < 4; ++e) {
        float s0 = 0.f;
#pragma unroll
        for (int u = 0; u < 4; ++u) s0 += svc[u * 256 + b * 16 + 4 * cq + e];
        sv[e] = s0;
      }
      put_granule(rs, G_Q + b * 1024 + 4 * qb + cq, pack2(sv[0], sv[1]), pack2(sv[2], sv[3]), tag);
    }
    {
      const int b = lane >> 3, col = lane & 7;
      float s0 = 0.f;
#pragma unroll
      for (int u = 0; u < 4; ++u) s0 += svc[u * 256 + 128 + b * 16 + col];
      const float t = bf16r(bf16r(s0) + bfu((u16)b1v));
      const float inner = 0.7978845608028654f * (t + 0.044715f * t * t * t);
      const float hv = 0.5f * t * (1.0f + tanhf(inner));
      const float h1 = dpp<XOR1>(hv);
      const u32 pr = (lane & 1) ? pack2(h1, hv) : pack2(hv, h1);
      const u32 pr2 = __builtin_bit_cast(u32, dpp<XOR2>(__builtin_bit_cast(float, pr)));
      if (h_on && (lane & 3) == 0) put_granule(rs, G_H + b * 448 + 2 * qb + (col >> 2), pr, pr2, tag);
    }
    STAMP(1);
    u32x4 gq[2], gh[2];
    for (u32 spins = 0; spins < SPIN_CAP; ++spins) {
      asm volatile("" ::: "memory");
      bool ok = true;
#pragma unroll
      for (int k = 0; k < 2; ++k) { gq[k] = get_granule(rs, G_Q + ab * 1024 + ahkv * 128 + 64 * k + lane); ok &= tag_ok(gq[k], tag); }
      if (__all(ok)) break;
      __builtin_amdgcn_s_sleep(1);
    }
    u64* qs = reinterpret_cast<u64*>(lds + L_Q);
#pragma unroll
    for (int k = 0; k < 2; ++k) qs[64 * k + lane] = ((u64)gq[k][1] << 32) | gq[k][0];
    asm volatile("s_waitcnt lgkmcnt(0)" ::: "memory");
    if (lane == 0) __hip_atomic_store(flags, 1u, __ATOMIC_RELAXED, __HIP_MEMORY_SCOPE_WORKGROUP);
    STAMP(2);
#pragma unroll
    for (int i = 0; i < 4 * NG; ++i) kv[i] = ld_nt(a.kc + kfbase + (size_t)(i >> 2) * 16 * DH + (i & 3) * 32);
    u32 hg[2]; int hidx[2];
#pragma unroll
    for (int k = 0; k < 2; ++k) {
      const int idx = min(lane + 64 * k, 111);
      const int b = idx / 14, t = idx % 14;
      hg[k] = G_H + b * 448 + 14 * hi + t;
      hidx[k] = b * 16 + t;
    }
    for (u32 spins = 0; spins < SPIN_CAP; ++spins) {
      asm volatile("" ::: "memory");
      bool ok = true;
#pragma unroll
      for (int k = 0; k < 2; ++k) { gh[k] = get_granule(rs, hg[k]); ok &= tag_ok(gh[k], tag); }
      if (__all(ok)) break;
      __builtin_amdgcn_s_sleep(1);
    }
    u64* hs = reinterpret_cast<u64*>(lds + L_H);
#pragma unroll
    for (int k = 0; k < 2; ++k) if (lane + 64 * k < 112) hs[hidx[k]] = ((u64)gh[k][1] << 32) | gh[k][0];
    if (lane < 16) hs[(lane >> 1) * 16 + 14 + (lane & 1)] = 0ull;
    lds_wait_ge(flags + 2, 4u);
    asm volatile("s_waitcnt lgkmcnt(0)" ::: "memory");
#pragma unroll
    for (int ct = 0; ct < 4; ++ct) {
      f32x4 acc = {0.f, 0.f, 0.f, 0.f};
#pragma unroll
      for (int s = 0; s < 2; ++s) {
        const u32x4 af = *reinterpret_cast<const u32x4*>(lds + L_H + c * 128 + (32 * s + 8 * r) * 2);
        s16x8 bf;
#pragma unroll
        for (int jj = 0; jj < 8; ++jj)
          bf[jj] = *reinterpret_cast<const short*>(lds + L_W2 + (32 * s + 8 * r + jj) * WROW + (16 * ct + c) * 2);
        acc = __builtin_amdgcn_mfma_f32_16x16x32_bf16(__builtin_bit_cast(s16x8, af), bf, acc, 0, 0, 0);
      }
#pragma unroll
      for (int k = 0; k < 4; ++k) {
        const float nb = dpp<XOR1>(acc[k]);
        if (lane < 32 && (c & 1) == 0)
          put_granule(rs, G_Y + ((cj * NB + (4 * r + k)) * 32 + hi) * 32 + 8 * ct + (c >> 1), __float_as_uint(acc[k]), __float_as_uint(nb), tag);
      }
    }
    STAMP(3);
  } else {
    __builtin_amdgcn_s_setprio(0);
    lds_wait_ge(flags, 1u);
  }

  s16x8 qf[4];
#pragma unroll
  for (int s = 0; s < 4; ++s) {
    u32x4 t = *reinterpret_cast<const u32x4*>(lds + L_Q + (c & 3) * 256 + 64 * s + 16 * r);
    if (c >= 4) t = u32x4{0u, 0u, 0u, 0u};
    qf[s] = __builtin_bit_cast(s16x8, t);
  }
  float sc[NG][4];
#pragma unroll
  for (int grp = 0; grp < NG; ++grp) {
    f32x4 d = {0.f, 0.f, 0.f, 0.f};
#pragma unroll
    for (int s = 0; s < 4; ++s) d = __builtin_amdgcn_mfma_f32_16x16x32_bf16(__builtin_bit_cast(s16x8, kv[4 * grp + s]), qf[s], d, 0, 0, 0);
#pragma unroll
    for (int k = 0; k < 4; ++k) sc[grp][k] = bf16r(bf16r(d[k]) * 0.08838834764831845f);
  }
  STAMP(48 + v);
  float m = sc[0][0];
#pragma unroll
  for (int grp = 0; grp < NG; ++grp)
#pragma unroll
    for (int k = 0; k < 4; ++k) m = fmaxf(m, sc[grp][k]);
  m = xrow16_max(m);
  float l = 0.f;
  {
    float* pl = reinterpret_cast<float*>(lds + L_P) + v * 576;
#pragma unroll
    for (int grp = 0; grp < NG; ++grp)
#pragma unroll
      for (int k = 0; k < 4; ++k) {
        const float p = __builtin_amdgcn_exp2f((sc[grp][k] - m) * LOG2E);
        l += p;
        if (c < 4) pl[(16 * grp + 4 * r + k) * 4 + c] = p;
      }
  }
  l = xrow16_sum(l);
  asm volatile("s_waitcnt lgkmcnt(0)" ::: "memory");

#define VAH_G 4
  constexpr int VAH = VAH_G;
  f32x2 acc2[16];
#pragma unroll
  for (int k = 0; k < 16; ++k) acc2[k] = f32x2{0.f, 0.f};
  {
    const f32x4* pl4 = reinterpret_cast<const f32x4*>(lds + L_P) + v * 144;
#pragma unroll
    for (int grp = 0; grp < NG; ++grp) {
#pragma unroll
      for (int gg = (grp == 0 ? VPG : VPG + VAH - 1 + grp); gg <= VPG + VAH - 1 + grp; ++gg) {
        if (gg < NG) {
#pragma unroll
          for (int t = 0; t < 4; ++t) kv[4 * (gg - VPG) + t] = ld_nt(a.vc + kvbase + (size_t)(4 * gg + t) * 4 * DH);
        }
      }
      __builtin_amdgcn_sched_barrier(0);
#pragma unroll
      for (int t = 0; t < 4; ++t) {
        const int i = 4 * grp + t;
        const f32x4 p = pl4[4 * i + r];
#pragma unroll
        for (int k = 0; k < 4; ++k) {
          const u32 vv = (i < 4 * VPG) ? vpre[i < 4 * VPG ? i : 0][k] : kv[i >= 4 * VPG ? i - 4 * VPG : 0][k];
          const f32x2 vf = {bflo(vv), bfhi(vv)};
#pragma unroll
          for (int g = 0; g < 4; ++g) acc2[g * 4 + k] = __builtin_elementwise_fma(f32x2{p[g], p[g]}, vf, acc2[g * 4 + k]);
        }
      }
      __builtin_amdgcn_sched_barrier(0);
    }
  }
  float acc[32];
#pragma unroll
  for (int k = 0; k < 32; ++k) acc[k] = xrow16_sum(acc2[k >> 1][k & 1]);
  STAMP(40 + v);
  {
    float* mrg = reinterpret_cast<float*>(lds + L_MRG) + v * 4 * 132;
    if (r == 0) {
#pragma unroll
      for (int g = 0; g < 4; ++g) {
        *reinterpret_cast<f32x4*>(mrg + g * 132 + 4 + 8 * c) = f32x4{acc[g * 8], acc[g * 8 + 1], acc[g * 8 + 2], acc[g * 8 + 3]};
        *reinterpret_cast<f32x4*>(mrg + g * 132 + 4 + 8 * c + 4) = f32x4{acc[g * 8 + 4], acc[g * 8 + 5], acc[g * 8 + 6], acc[g * 8 + 7]};
      }
      if (c < 4) { mrg[c * 132] = m; mrg[c * 132 + 1] = l; }
    }
  }
  if (ROLE == 0 && hi < 8) {
    const u32 yg = G_Y + (cj * NB + hi) * 32 * 32 + (lane & 31) + (lane >> 5) * 32;
    float y0 = 0.f, y1 = 0.f;
    for (u32 spins = 0; spins < SPIN_CAP; ++spins) {
      asm volatile("" ::: "memory");
      bool ok = true; y0 = 0.f; y1 = 0.f;
#pragma unroll
      for (int i = 0; i < 16; ++i) { const u32x4 g = get_granule(rs, yg + 64 * i); y0 += __uint_as_float(g[0]); y1 += __uint_as_float(g[1]); ok &= tag_ok(g, tag); }
      if (__all(ok)) break;
      __builtin_amdgcn_s_sleep(8);
    }
    {
      auto t0 = __builtin_amdgcn_permlane32_swap(__float_as_uint(y0), __float_as_uint(y0), false, false);
      y0 = __uint_as_float(t0[0]) + __uint_as_float(t0[1]);
      auto t1 = __builtin_amdgcn_permlane32_swap(__float_as_uint(y1), __float_as_uint(y1), false, false);
      y1 = __uint_as_float(t1[0]) + __uint_as_float(t1[1]);
    }
    if (lane < 32) {
      const u32 b2p = *reinterpret_cast<const u32*>(a.b2 + 64 * cj + 2 * lane);
      const float ya = bf16r(bf16r(y0) + bflo(b2p)), yb = bf16r(bf16r(y1) + bfhi(b2p));
      reinterpret_cast<float*>(lds + L_G)[2 * lane] = bf16r(ya / (1.0f + __expf(-ya)));
      reinterpret_cast<float*>(lds + L_G)[2 * lane + 1] = bf16r(yb / (1.0f + __expf(-yb)));
    }
  }
  wg_barrier();
  if (v == 0) STAMP(7);

  if (tid < 256) {
    const int g = tid >> 6, d2 = tid & 63;
    const float* mrg = reinterpret_cast<const float*>(lds + L_MRG) + g * 132;
    float M = mrg[0];
#pragma unroll
    for (int u = 1; u < 8; ++u) M = fmaxf(M, mrg[u * 4 * 132]);
    float L = 0.f, o0 = 0.f, o1 = 0.f;
#pragma unroll
    for (int u = 0; u < 8; ++u) {
      const float wt = __builtin_amdgcn_exp2f((mrg[u * 4 * 132] - M) * LOG2E);
      L = fmaf(wt, mrg[u * 4 * 132 + 1], L);
      o0 = fmaf(wt, mrg[u * 4 * 132 + 4 + 2 * d2], o0);
      o1 = fmaf(wt, mrg[u * 4 * 132 + 5 + 2 * d2], o1);
    }
    const u32 pg = G_P + ((ab * HQ + ahkv * 4 + g) * 4 + split) * 66;
    put_granule(rs, pg + d2, __float_as_uint(o0), __float_as_uint(o1), tag);
    if (d2 == 0) put_granule(rs, pg + 64, __float_as_uint(M), __float_as_uint(L), tag);
  }

  {
    const u32 pg = G_P + (v * HQ + hi) * 4 * 66;
    probe_wait(rs, pg + (lane & 3) * 66 + 64, lane < 4, tag);
    u32x4 go[4], gm[4];
    for (u32 spins = 0; spins < SPIN_CAP; ++spins) {
      asm volatile("" ::: "memory");
      bool ok = true;
#pragma unroll
      for (int s = 0; s < 4; ++s) {
        go[s] = get_granule(rs, pg + s * 66 + lane);
        gm[s] = get_granule(rs, pg + s * 66 + 64);
        ok &= tag_ok(go[s], tag) & tag_ok(gm[s], tag);
      }
      if (__all(ok)) break;
      __builtin_amdgcn_s_sleep(2);
    }
    float M = __uint_as_float(gm[0][0]);
#pragma unroll
    for (int s = 1; s < 4; ++s) M = fmaxf(M, __uint_as_float(gm[s][0]));
    float L = 0.f, a0 = 0.f, a1 = 0.f;
#pragma unroll
    for (int s = 0; s < 4; ++s) {
      const float wt = __builtin_amdgcn_exp2f((__uint_as_float(gm[s][0]) - M) * LOG2E);
      L = fmaf(wt, __uint_as_float(gm[s][1]), L);
      a0 = fmaf(wt, __uint_as_float(go[s][0]), a0);
      a1 = fmaf(wt, __uint_as_float(go[s][1]), a1);
    }
    const float inv = 1.0f / L;
    reinterpret_cast<u32*>(lds + L_O)[v * 64 + lane] = pack2(a0 * inv, a1 * inv);
  }
  wg_barrier();
  if (v == 0) STAMP(8);
  if (ROLE <= 1) {
    const int ct = v;
    f32x4 acc4 = {0.f, 0.f, 0.f, 0.f};
#pragma unroll
    for (int s = 0; s < 4; ++s) {
      const u32x4 af = *reinterpret_cast<const u32x4*>(lds + L_O + c * 256 + (32 * s + 8 * r) * 2);
      s16x8 bf;
#pragma unroll
      for (int jj = 0; jj < 8; ++jj)
        bf[jj] = *reinterpret_cast<const short*>(lds + L_WO + (32 * s + 8 * r + jj) * WROW + (16 * ct + c) * 2);
      acc4 = __builtin_amdgcn_mfma_f32_16x16x32_bf16(__builtin_bit_cast(s16x8, af), bf, acc4, 0, 0, 0);
    }
#pragma unroll
    for (int k = 0; k < 4; ++k) {
      const float nb = dpp<XOR1>(acc4[k]);
      if (lane < 32 && (c & 1) == 0)
        put_granule(rs, G_S + ((cj * NB + (4 * r + k)) * 32 + hi) * 32 + 8 * ct + (c >> 1), __float_as_uint(acc4[k]), __float_as_uint(nb), tag);
    }
  }
  if (ROLE == 2 && hi < 8 && v == 4) {
    const u32 sg = G_S + (cj * NB + hi) * 32 * 32;
    float s0 = 0.f, s1 = 0.f;
    for (u32 spins = 0; spins < SPIN_CAP; ++spins) {
      asm volatile("" ::: "memory");
      bool ok = true; s0 = 0.f; s1 = 0.f;
#pragma unroll
      for (int i = 0; i < 16; ++i) { const u32x4 g = get_granule(rs, sg + lane + 64 * i); s0 += __uint_as_float(g[0]); s1 += __uint_as_float(g[1]); ok &= tag_ok(g, tag); }
      if (__all(ok)) break;
      __builtin_amdgcn_s_sleep(2);
    }
    {
      auto t0 = __builtin_amdgcn_permlane32_swap(__float_as_uint(s0), __float_as_uint(s0), false, false);
      s0 = __uint_as_float(t0[0]) + __uint_as_float(t0[1]);
      auto t1 = __builtin_amdgcn_permlane32_swap(__float_as_uint(s1), __float_as_uint(s1), false, false);
      s1 = __uint_as_float(t1[0]) + __uint_as_float(t1[1]);
    }
    if (lane < 32) {
      const float g0 = reinterpret_cast<const float*>(lds + L_G)[2 * lane], g1 = reinterpret_cast<const float*>(lds + L_G)[2 * lane + 1];
      const u32 xp = *reinterpret_cast<const u32*>(a.x + hi * DM + 64 * cj + 2 * lane);
      const float r0 = bflo(xp) + bf16r(bf16r(s0) * g0), r1 = bfhi(xp) + bf16r(bf16r(s1) * g1);
      *reinterpret_cast<u32*>(a.out + hi * DM + 64 * cj + 2 * lane) = pack2(r0, r1);
    }
    if (w == 0 && lane == 0)
      __hip_atomic_store(a.ws, ((u64)tag.hi << 32) | tag.lo, __ATOMIC_RELAXED, __HIP_MEMORY_SCOPE_AGENT);
    STAMP(9);
  }
}

__global__ void __launch_bounds__(NTHR, 2) fused_decode_kernel(Params a) {
  __shared__ __attribute__((aligned(16))) unsigned char lds[L_END];
  const int v = __builtin_amdgcn_readfirstlane(threadIdx.x >> 6);
  if (v == 0) body<0>(a, lds, 0);
  else if (v < 4) body<1>(a, lds, v);
  else body<2>(a, lds, v);
}

extern "C" void kernel_launch(void* const* d_in, const int* in_sizes, int n_in,
                              void* d_out, int out_size, void* d_ws, size_t ws_size,
                              hipStream_t stream) {
  Params p;
  p.x = (const u16*)d_in[0];
  p.kc = (const u16*)d_in[1];
  p.vc = (const u16*)d_in[2];
  p.wq = (const u16*)d_in[3];
  p.wo = (const u16*)d_in[4];
  p.w1 = (const u16*)d_in[6];
  p.b1 = (const u16*)d_in[7];
  p.w2 = (const u16*)d_in[8];
  p.b2 = (const u16*)d_in[9];
  p.out = (u16*)d_out;
  p.ws = (u64*)d_ws;
  hipLaunchKernelGGL(fused_decode_kernel, dim3(NWG), dim3(NTHR), 0, stream, p);
}
```

```cpp
#include <hip/hip_runtime.h>
#include <hip/hip_bf16.h>
#include <cstdint>

typedef unsigned int u32;
typedef unsigned long long u64;
typedef unsigned short u16;
using u32x4 = __attribute__((ext_vector_type(4))) u32;
using f32x4 = __attribute__((ext_vector_type(4))) float;
using f32x2 = __attribute__((ext_vector_type(2))) float;
using s16x8 = __attribute__((ext_vector_type(8))) short;
using bf16x2 = __attribute__((ext_vector_type(2))) __bf16;

constexpr int NB = 8, DM = 512, HQ = 32, HKV = 8, SK = 4096, DH = 128, FF = 1792;
constexpr int NWG = 256, NTHR = 512;
constexpr u32 SPIN_CAP = 1u << 20;
constexpr float LOG2E = 1.4426950408889634f;

constexpr u32 G_Q = 16;
constexpr u32 G_H = G_Q + NB * 1024;
constexpr u32 G_P = G_H + NB * 448;
constexpr u32 G_S = G_P + NB * HQ * 4 * 66;
constexpr u32 G_Y = G_S + 8 * NB * 32 * 32;
constexpr u32 G_END = G_Y + 8 * NB * 32 * 32;

__device__ __forceinline__ float bf16r(float x) { return (float)(__bf16)x; }
__device__ __forceinline__ u32 pack2(float lo, float hi) {
  bf16x2 v = {(__bf16)lo, (__bf16)hi};
  return __builtin_bit_cast(u32, v);
}
__device__ __forceinline__ float bflo(u32 x) { return __builtin_bit_cast(float, x << 16); }
__device__ __forceinline__ float bfhi(u32 x) { return __builtin_bit_cast(float, x & 0xffff0000u); }
__device__ __forceinline__ float bfu(u16 x) { return __builtin_bit_cast(float, (u32)x << 16); }
__device__ __forceinline__ float dot2(u32 k, u32 q, float acc) {
  return __builtin_amdgcn_fdot2_f32_bf16(__builtin_bit_cast(bf16x2, k), __builtin_bit_cast(bf16x2, q), acc, false);
}
template <int CTRL> __device__ __forceinline__ float dpp(float x) {
  return __builtin_bit_cast(float, __builtin_amdgcn_mov_dpp(__builtin_bit_cast(int, x), CTRL, 0xf, 0xf, true));
}
constexpr int XOR1 = 0xB1, XOR2 = 0x4E, XOR7 = 0x141, XOR8 = 0x128, ROR4 = 0x124;
__device__ __forceinline__ float xrow16_max(float x) {
  auto s = __builtin_amdgcn_permlane16_swap(__float_as_uint(x), __float_as_uint(x), false, false);
  x = fmaxf(__uint_as_float(s[0]), __uint_as_float(s[1]));
  auto t = __builtin_amdgcn_permlane32_swap(__float_as_uint(x), __float_as_uint(x), false, false);
  return fmaxf(__uint_as_float(t[0]), __uint_as_float(t[1]));
}
__device__ __forceinline__ float xrow16_sum(float x) {
  auto s = __builtin_amdgcn_permlane16_swap(__float_as_uint(x), __float_as_uint(x), false, false);
  x = __uint_as_float(s[0]) + __uint_as_float(s[1]);
  auto t = __builtin_amdgcn_permlane32_swap(__float_as_uint(x), __float_as_uint(x), false, false);
  return __uint_as_float(t[0]) + __uint_as_float(t[1]);
}
typedef __amdgpu_buffer_rsrc_t rsrc_t;
struct Tag { u32 lo, hi; };
__device__ __forceinline__ void put_granule(rsrc_t rs, u32 g, u32 v0, u32 v1, Tag t) {
  __builtin_amdgcn_raw_buffer_store_b128(u32x4{v0, v1, t.lo, t.hi}, rs, (int)(g * 16u), 0, 16);
}
__device__ __forceinline__ u32x4 get_granule(rsrc_t rs, u32 g) {
  return __builtin_amdgcn_raw_buffer_load_b128(rs, (int)(g * 16u), 0, 16);
}
__device__ __forceinline__ bool tag_ok(u32x4 g, Tag t) { return (g[2] == t.lo) & (g[3] == t.hi); }
__device__ __forceinline__ void wg_barrier() {
  asm volatile("s_waitcnt lgkmcnt(0)" ::: "memory");
  __builtin_amdgcn_s_barrier();
  asm volatile("" ::: "memory");
}
__device__ __forceinline__ u32x4 ld_nt(const u16* p) {
  return __builtin_nontemporal_load(reinterpret_cast<const u32x4*>(p));
}
__device__ __forceinline__ u32 lds_flag_load(u32* f) {
  return (u32)__builtin_amdgcn_readfirstlane(__hip_atomic_load(f, __ATOMIC_RELAXED, __HIP_MEMORY_SCOPE_WORKGROUP));
}
__device__ __forceinline__ void lds_wait_ge(u32* f, u32 want) {
  for (u32 spins = 0; spins < SPIN_CAP * 8u; ++spins) {
    if (lds_flag_load(f) >= want) break;
    __builtin_amdgcn_s_sleep(1);
  }
  asm volatile("" ::: "memory");
}
__device__ __forceinline__ void probe_wait(rsrc_t rs, u32 g, bool active, Tag t) {
  for (u32 spins = 0; spins < SPIN_CAP; ++spins) {
    asm volatile("" ::: "memory");
    bool ok = true;
    if (active) ok = tag_ok(get_granule(rs, g), t);
    if (__all(ok)) break;
    __builtin_amdgcn_s_sleep(2);
  }
}

#define STAMP(k) do { } while (0)

struct Params {
  const u16* x; const u16* kc; const u16* vc; const u16* wq; const u16* wo;
  const u16* w1; const u16* b1; const u16* w2; const u16* b2;
  u16* out; u64* ws;
};

constexpr int WROW = 144;
constexpr int L_WO = 0;
constexpr int L_W2 = L_WO + 128 * WROW;
constexpr int L_SVC = L_W2 + 64 * WROW;
constexpr int L_Q = L_SVC + 4 * 2 * 8 * 16 * 4;
constexpr int L_H = L_Q + 1024;
constexpr int L_O = L_H + 16 * 32 * 4;
constexpr int L_G = L_O + 16 * 64 * 4;
constexpr int L_FLAG = L_G + 256;
constexpr int L_P = L_FLAG + 32;
constexpr int L_MRG = L_P + 8 * 144 * 4 * 4;
constexpr int L_WQS = L_MRG + 8 * 4 * 132 * 4;
constexpr int L_W1S = L_WQS + 4 * 4352;
constexpr int L_XN = L_W1S + 4 * 2304;
constexpr int L_END = L_XN + 64;

template <int ROLE>
__device__ __forceinline__ void body(const Params& a, unsigned char* lds, const int v) {
  constexpr int NG = (ROLE == 0) ? 1 : 9;
  const int w = blockIdx.x;
  const int tid = threadIdx.x, lane = tid & 63;
  const int r = lane >> 4, c = lane & 15;
  const int bh = w >> 2, split = w & 3;
  const int ab = bh >> 3, ahkv = bh & 7;
  const int hi = w >> 3, cj = w & 7;
  const int qb = (w & 7) * 32 + (w >> 3);
  const bool h_on = (qb < 224);
  const rsrc_t rs = __builtin_amdgcn_make_buffer_rsrc((void*)a.ws, 0, (int)(G_END * 16u), 0x00020000);
  Tag tag = {0u, 0u};
  u64 epoch_raw = 0;
  if (ROLE == 0) epoch_raw = __hip_atomic_load(a.ws, __ATOMIC_RELAXED, __HIP_MEMORY_SCOPE_AGENT);
  u32* flags = reinterpret_cast<u32*>(lds + L_FLAG);
  STAMP(16 + v);

  if (ROLE == 0) {
    if (lane < 6) flags[lane] = 0u;
  }
  wg_barrier();
  __builtin_amdgcn_s_setprio(3);

  const int key0 = (ROLE == 0) ? 0 : 16 + 144 * (v - 1);
  const size_t kvbase = ((size_t)bh * SK + split * 1024 + key0) * DH + (size_t)r * DH + 8 * c;
  const size_t kfbase = ((size_t)bh * SK + split * 1024 + key0) * DH + (size_t)c * DH + 8 * r;
#define VPRE_G 3
  constexpr int VPG = (ROLE == 0) ? 0 : VPRE_G;
  u32x4 kv[4 * NG];
  u32x4 vpre[4 * VPG + 1];
  u32 b1v = 0;

  if (ROLE <= 1) {
    u32x4 xs[2], xa[4];
#pragma unroll
    for (int i = 0; i < 2; ++i) xs[i] = ld_nt(a.x + (2 * v + i) * DM + 8 * lane);
#pragma unroll
    for (int s = 0; s < 4; ++s) xa[s] = ld_nt(a.x + (c & 7) * DM + 128 * v + 32 * s + 8 * r);
    u32x4 wqr[4], w1r[2];
#pragma unroll
    for (int u = 0; u < 4; ++u)
      wqr[u] = ld_nt(a.wq + (size_t)(128 * v + 32 * u + (lane >> 1)) * 4096 + 16 * qb + 8 * (lane & 1));
#pragma unroll
    for (int u = 0; u < 2; ++u)
      w1r[u] = ld_nt(a.w1 + (size_t)(128 * v + 64 * u + lane) * FF + 8 * (h_on ? qb : 0));
    if (ROLE == 0) b1v = a.b1[8 * (h_on ? qb : 0) + (lane & 7)];
    __builtin_amdgcn_sched_barrier(0);
    if (lane == 0) __hip_atomic_fetch_add(flags + 3, 1u, __ATOMIC_RELAXED, __HIP_MEMORY_SCOPE_WORKGROUP);
    float* stat = reinterpret_cast<float*>(lds + L_XN);
#pragma unroll
    for (int i = 0; i < 2; ++i) {
      float s = 0.f;
#pragma unroll
      for (int k = 0; k < 4; ++k) s += bflo(xs[i][k]) + bfhi(xs[i][k]);
      s += dpp<XOR1>(s); s += dpp<XOR2>(s); s += dpp<XOR7>(s); s += dpp<XOR8>(s); s = xrow16_sum(s);
      const float mean = s * (1.0f / DM);
      float q2 = 0.f;
#pragma unroll
      for (int k = 0; k < 4; ++k) {
        const float d0 = bflo(xs[i][k]) - mean, d1 = bfhi(xs[i][k]) - mean;
        q2 = fmaf(d0, d0, q2); q2 = fmaf(d1, d1, q2);
      }
      q2 += dpp<XOR1>(q2); q2 += dpp<XOR2>(q2); q2 += dpp<XOR7>(q2); q2 += dpp<XOR8>(q2); q2 = xrow16_sum(q2);
      const float var_bf = bf16r(q2 * (1.0f / DM));
      if (lane == 0) { stat[4 * v + 2 * i] = bf16r(mean); stat[4 * v + 2 * i + 1] = bf16r(sqrtf(bf16r(var_bf + 1e-5f))); }
    }
    asm volatile("s_waitcnt lgkmcnt(0)" ::: "memory");
    if (lane == 0) __hip_atomic_fetch_add(flags + 4, 1u, __ATOMIC_RELAXED, __HIP_MEMORY_SCOPE_WORKGROUP);
    if (v == 1) STAMP(12);
    unsigned char* wqs = lds + L_WQS + v * 4352;
    unsigned char* w1s = lds + L_W1S + v * 2304;
#pragma unroll
    for (int u = 0; u < 4; ++u) {
      const int rr = 32 * u + (lane >> 1);
      *reinterpret_cast<u32x4*>(wqs + rr * 32 + (rr >> 3) * 16 + 16 * (lane & 1)) = wqr[u];
    }
#pragma unroll
    for (int u = 0; u < 2; ++u) {
      const int rr = 64 * u + lane;
      *reinterpret_cast<u32x4*>(w1s + rr * 16 + (rr >> 3) * 16) = w1r[u];
    }
    lds_wait_ge(flags + 4, 4u);
    const float mean_bf = stat[2 * (c & 7)], sd_bf = stat[2 * (c & 7) + 1];
    asm volatile("s_waitcnt lgkmcnt(0)" ::: "memory");
    f32x4 accq = {0.f, 0.f, 0.f, 0.f}, acch = {0.f, 0.f, 0.f, 0.f};
#pragma unroll
    for (int s = 0; s < 4; ++s) {
      u32x4 af;
#pragma unroll
      for (int k = 0; k < 4; ++k)
        af[k] = (c < 8) ? pack2(bf16r(bflo(xa[s][k]) - mean_bf) / sd_bf, bf16r(bfhi(xa[s][k]) - mean_bf) / sd_bf) : 0u;
      const s16x8 afr = __builtin_bit_cast(s16x8, af);
      s16x8 bq, bws;
#pragma unroll
      for (int jj = 0; jj < 8; ++jj) {
        const int k = 32 * s + 8 * r + jj;
        bq[jj] = *reinterpret_cast<const short*>(wqs + k * 32 + (k >> 3) * 16 + c * 2);
        bws[jj] = *reinterpret_cast<const short*>(w1s + k * 16 + (k >> 3) * 16 + (c & 7) * 2);
      }
      if (c >= 8) bws = s16x8{0, 0, 0, 0, 0, 0, 0, 0};
      accq = __builtin_amdgcn_mfma_f32_16x16x32_bf16(afr, bq, accq, 0, 0, 0);
      acch = __builtin_amdgcn_mfma_f32_16x16x32_bf16(afr, bws, acch, 0, 0, 0);
    }
    if (v == 1) STAMP(13);
    float* svc = reinterpret_cast<float*>(lds + L_SVC) + v * 256;
    if (lane < 32) {
#pragma unroll
      for (int k = 0; k < 4; ++k) {
        svc[(4 * r + k) * 16 + c] = accq[k];
        svc[128 + (4 * r + k) * 16 + c] = acch[k];
      }
    }
    if (ROLE == 1) {
      asm volatile("s_waitcnt lgkmcnt(0)" ::: "memory");
      if (lane == 0) __hip_atomic_fetch_add(flags + 1, 1u, __ATOMIC_RELAXED, __HIP_MEMORY_SCOPE_WORKGROUP);
    }
  }
  STAMP(24 + v);

  if (ROLE == 2) {
    const int t4 = tid - 256;
    lds_wait_ge(flags + 3, 4u);
    u32x4 st[6];
#pragma unroll
    for (int u = 0; u < 4; ++u) {
      const int e = t4 + 256 * u, row = e >> 3, seg = e & 7;
      st[u] = ld_nt(a.wo + (size_t)(128 * hi + row) * DM + 64 * cj + 8 * seg);
    }
#pragma unroll
    for (int u = 0; u < 2; ++u) {
      const int e = t4 + 256 * u, row = e >> 3, seg = e & 7;
      st[4 + u] = ld_nt(a.w2 + (size_t)(56 * hi + (row < 56 ? row : 0)) * DM + 64 * cj + 8 * seg);
    }
#pragma unroll
    for (int i = 0; i < 4 * NG; ++i) kv[i] = ld_nt(a.kc + kfbase + (size_t)(i >> 2) * 16 * DH + (i & 3) * 32);
#pragma unroll
    for (int i = 0; i < 4 * VPG; ++i) vpre[i] = ld_nt(a.vc + kvbase + (size_t)i * 4 * DH);
    __builtin_amdgcn_sched_barrier(0);
#pragma unroll
    for (int u = 0; u < 4; ++u) {
      const int e = t4 + 256 * u, row = e >> 3, seg = e & 7;
      *reinterpret_cast<u32x4*>(lds + L_WO + row * WROW + 16 * seg) = st[u];
    }
#pragma unroll
    for (int u = 0; u < 2; ++u) {
      const int e = t4 + 256 * u, row = e >> 3, seg = e & 7;
      *reinterpret_cast<u32x4*>(lds + L_W2 + row * WROW + 16 * seg) = row < 56 ? st[4 + u] : u32x4{0u, 0u, 0u, 0u};
    }
    reinterpret_cast<u32*>(lds + L_H)[256 + t4] = 0u;
    reinterpret_cast<u32*>(lds + L_O)[512 + t4] = 0u;
    reinterpret_cast<u32*>(lds + L_O)[768 + t4] = 0u;
    asm volatile("s_waitcnt lgkmcnt(0)" ::: "memory");
    if (lane == 0) __hip_atomic_fetch_add(flags + 2, 1u, __ATOMIC_RELAXED, __HIP_MEMORY_SCOPE_WORKGROUP);
  }
  if (ROLE == 1) {
#pragma unroll
    for (int i = 0; i < 4 * NG; ++i) kv[i] = ld_nt(a.kc + kfbase + (size_t)(i >> 2) * 16 * DH + (i & 3) * 32);
#pragma unroll
    for (int i = 0; i < 4 * VPG; ++i) vpre[i] = ld_nt(a.vc + kvbase + (size_t)i * 4 * DH);
    __builtin_amdgcn_sched_barrier(0);
  }

  if (ROLE == 0) {
    lds_wait_ge(flags + 1, 3u);
    {
      const u64 e = epoch_raw + 1ull;
      tag.lo = (u32)__builtin_amdgcn_readfirstlane((u32)e);
      tag.hi = (u32)__builtin_amdgcn_readfirstlane((u32)(e >> 32));
      if (lane == 0) { flags[6] = tag.lo; flags[7] = tag.hi; }
    }
    const float* svc = reinterpret_cast<const float*>(lds + L_SVC);
    if (lane < 32) {
      const int b = lane >> 2, cq = lane & 3;
      float sv[4];
#pragma unroll
      for (int e = 0; e < 4; ++e) {
        float s0 = 0.f;
#pragma unroll
        for (int u = 0; u < 4; ++u) s0 += svc[u * 256 + b * 16 + 4 * cq + e];
        sv[e] = s0;
      }
      put_granule(rs, G_Q + b * 1024 + 4 * qb + cq, pack2(sv[0], sv[1]), pack2(sv[2], sv[3]), tag);
    }
    {
      const int b = lane >> 3, col = lane & 7;
      float s0 = 0.f;
#pragma unroll
      for (int u = 0; u < 4; ++u) s0 += svc[u * 256 + 128 + b * 16 + col];
      const float t = bf16r(bf16r(s0) + bfu((u16)b1v));
      const float inner = 0.7978845608028654f * (t + 0.044715f * t * t * t);
      const float hv = 0.5f * t * (1.0f + tanhf(inner));
      const float h1 = dpp<XOR1>(hv);
      const u32 pr = (lane & 1) ? pack2(h1, hv) : pack2(hv, h1);
      const u32 pr2 = __builtin_bit_cast(u32, dpp<XOR2>(__builtin_bit_cast(float, pr)));
      if (h_on && (lane & 3) == 0) put_granule(rs, G_H + b * 448 + 2 * qb + (col >> 2), pr, pr2, tag);
    }
    STAMP(1);
    u32x4 gq[2], gh[2];
    for (u32 spins = 0; spins < SPIN_CAP; ++spins) {
      asm volatile("" ::: "memory");
      bool ok = true;
#pragma unroll
      for (int k = 0; k < 2; ++k) { gq[k] = get_granule(rs, G_Q + ab * 1024 + ahkv * 128 + 64 * k + lane); ok &= tag_ok(gq[k], tag); }
      if (__all(ok)) break;
      __builtin_amdgcn_s_sleep(1);
    }
    u64* qs = reinterpret_cast<u64*>(lds + L_Q);
#pragma unroll
    for (int k = 0; k < 2; ++k) qs[64 * k + lane] = ((u64)gq[k][1] << 32) | gq[k][0];
    asm volatile("s_waitcnt lgkmcnt(0)" ::: "memory");
    if (lane == 0) __hip_atomic_store(flags, 1u, __ATOMIC_RELAXED, __HIP_MEMORY_SCOPE_WORKGROUP);
    STAMP(2);
#pragma unroll
    for (int i = 0; i < 4 * NG; ++i) kv[i] = ld_nt(a.kc + kfbase + (size_t)(i >> 2) * 16 * DH + (i & 3) * 32);
    u32 hg[2]; int hidx[2];
#pragma unroll
    for (int k = 0; k < 2; ++k) {
      const int idx = min(lane + 64 * k, 111);
      const int b = idx / 14, t = idx % 14;
      hg[k] = G_H + b * 448 + 14 * hi + t;
      hidx[k] = b * 16 + t;
    }
    for (u32 spins = 0; spins < SPIN_CAP; ++spins) {
      asm volatile("" ::: "memory");
      bool ok = true;
#pragma unroll
      for (int k = 0; k < 2; ++k) { gh[k] = get_granule(rs, hg[k]); ok &= tag_ok(gh[k], tag); }
      if (__all(ok)) break;
      __builtin_amdgcn_s_sleep(1);
    }
    u64* hs = reinterpret_cast<u64*>(lds + L_H);
#pragma unroll
    for (int k = 0; k < 2; ++k) if (lane + 64 * k < 112) hs[hidx[k]] = ((u64)gh[k][1] << 32) | gh[k][0];
    if (lane < 16) hs[(lane >> 1) * 16 + 14 + (lane & 1)] = 0ull;
    lds_wait_ge(flags + 2, 4u);
    asm volatile("s_waitcnt lgkmcnt(0)" ::: "memory");
#pragma unroll
    for (int ct = 0; ct < 4; ++ct) {
      f32x4 acc = {0.f, 0.f, 0.f, 0.f};
#pragma unroll
      for (int s = 0; s < 2; ++s) {
        const u32x4 af = *reinterpret_cast<const u32x4*>(lds + L_H + c * 128 + (32 * s + 8 * r) * 2);
        s16x8 bf;
#pragma unroll
        for (int jj = 0; jj < 8; ++jj)
          bf[jj] = *reinterpret_cast<const short*>(lds + L_W2 + (32 * s + 8 * r + jj) * WROW + (16 * ct + c) * 2);
        acc = __builtin_amdgcn_mfma_f32_16x16x32_bf16(__builtin_bit_cast(s16x8, af), bf, acc, 0, 0, 0);
      }
#pragma unroll
      for (int k = 0; k < 4; ++k) {
        const float nb = dpp<XOR1>(acc[k]);
        if (lane < 32 && (c & 1) == 0)
          put_granule(rs, G_Y + ((cj * NB + (4 * r + k)) * 32 + hi) * 32 + 8 * ct + (c >> 1), __float_as_uint(acc[k]), __float_as_uint(nb), tag);
      }
    }
    STAMP(3);
  } else {
    __builtin_amdgcn_s_setprio(0);
    lds_wait_ge(flags, 1u);
  }

  s16x8 qf[4];
#pragma unroll
  for (int s = 0; s < 4; ++s) {
    u32x4 t = *reinterpret_cast<const u32x4*>(lds + L_Q + (c & 3) * 256 + 64 * s + 16 * r);
    if (c >= 4) t = u32x4{0u, 0u, 0u, 0u};
    qf[s] = __builtin_bit_cast(s16x8, t);
  }
  float sc[NG][4];
#pragma unroll
  for (int grp = 0; grp < NG; ++grp) {
    f32x4 d = {0.f, 0.f, 0.f, 0.f};
#pragma unroll
    for (int s = 0; s < 4; ++s) d = __builtin_amdgcn_mfma_f32_16x16x32_bf16(__builtin_bit_cast(s16x8, kv[4 * grp + s]), qf[s], d, 0, 0, 0);
#pragma unroll
    for (int k = 0; k < 4; ++k) sc[grp][k] = bf16r(bf16r(d[k]) * 0.08838834764831845f);
  }
  STAMP(48 + v);
  float m = sc[0][0];
#pragma unroll
  for (int grp = 0; grp < NG; ++grp)
#pragma unroll
    for (int k = 0; k < 4; ++k) m = fmaxf(m, sc[grp][k]);
  m = xrow16_max(m);
  float l = 0.f;
  {
    float* pl = reinterpret_cast<float*>(lds + L_P) + v * 576;
#pragma unroll
    for (int grp = 0; grp < NG; ++grp)
#pragma unroll
      for (int k = 0; k < 4; ++k) {
        const float p = __builtin_amdgcn_exp2f((sc[grp][k] - m) * LOG2E);
        l += p;
        if (c < 4) pl[(16 * grp + 4 * r + k) * 4 + c] = p;
      }
  }
  l = xrow16_sum(l);
  asm volatile("s_waitcnt lgkmcnt(0)" ::: "memory");

#define VAH_G 3
  constexpr int VAH = VAH_G;
  f32x2 acc2[16];
#pragma unroll
  for (int k = 0; k < 16; ++k) acc2[k] = f32x2{0.f, 0.f};
  {
    const f32x4* pl4 = reinterpret_cast<const f32x4*>(lds + L_P) + v * 144;
#pragma unroll
    for (int grp = 0; grp < NG; ++grp) {
#pragma unroll
      for (int gg = (grp == 0 ? VPG : VPG + VAH - 1 + grp); gg <= VPG + VAH - 1 + grp; ++gg) {
        if (gg < NG) {
#pragma unroll
          for (int t = 0; t < 4; ++t) kv[4 * (gg - VPG) + t] = ld_nt(a.vc + kvbase + (size_t)(4 * gg + t) * 4 * DH);
        }
      }
      __builtin_amdgcn_sched_barrier(0);
#pragma unroll
      for (int t = 0; t < 4; ++t) {
        const int i = 4 * grp + t;
        const f32x4 p = pl4[4 * i + r];
#pragma unroll
        for (int k = 0; k < 4; ++k) {
          const u32 vv = (i < 4 * VPG) ? vpre[i < 4 * VPG ? i : 0][k] : kv[i >= 4 * VPG ? i - 4 * VPG : 0][k];
          const f32x2 vf = {bflo(vv), bfhi(vv)};
#pragma unroll
          for (int g = 0; g < 4; ++g) acc2[g * 4 + k] = __builtin_elementwise_fma(f32x2{p[g], p[g]}, vf, acc2[g * 4 + k]);
        }
      }
      __builtin_amdgcn_sched_barrier(0);
    }
  }
  float acc[32];
#pragma unroll
  for (int k = 0; k < 32; ++k) acc[k] = xrow16_sum(acc2[k >> 1][k & 1]);
  STAMP(40 + v);
  {
    float* mrg = reinterpret_cast<float*>(lds + L_MRG) + v * 4 * 132;
    if (r == 0) {
#pragma unroll
      for (int g = 0; g < 4; ++g) {
        *reinterpret_cast<f32x4*>(mrg + g * 132 + 4 + 8 * c) = f32x4{acc[g * 8], acc[g * 8 + 1], acc[g * 8 + 2], acc[g * 8 + 3]};
        *reinterpret_cast<f32x4*>(mrg + g * 132 + 4 + 8 * c + 4) = f32x4{acc[g * 8 + 4], acc[g * 8 + 5], acc[g * 8 + 6], acc[g * 8 + 7]};
      }
      if (c < 4) { mrg[c * 132] = m; mrg[c * 132 + 1] = l; }
    }
  }
  if (ROLE == 0 && hi < 8) {
    const u32 yg = G_Y + (cj * NB + hi) * 32 * 32 + (lane & 31) + (lane >> 5) * 32;
    float y0 = 0.f, y1 = 0.f;
    for (u32 spins = 0; spins < SPIN_CAP; ++spins) {
      asm volatile("" ::: "memory");
      bool ok = true; y0 = 0.f; y1 = 0.f;
#pragma unroll
      for (int i = 0; i < 16; ++i) { const u32x4 g = get_granule(rs, yg + 64 * i); y0 += __uint_as_float(g[0]); y1 += __uint_as_float(g[1]); ok &= tag_ok(g, tag); }
      if (__all(ok)) break;
      __builtin_amdgcn_s_sleep(8);
    }
    {
      auto t0 = __builtin_amdgcn_permlane32_swap(__float_as_uint(y0), __float_as_uint(y0), false, false);
      y0 = __uint_as_float(t0[0]) + __uint_as_float(t0[1]);
      auto t1 = __builtin_amdgcn_permlane32_swap(__float_as_uint(y1), __float_as_uint(y1), false, false);
      y1 = __uint_as_float(t1[0]) + __uint_as_float(t1[1]);
    }
    if (lane < 32) {
      const u32 b2p = *reinterpret_cast<const u32*>(a.b2 + 64 * cj + 2 * lane);
      const float ya = bf16r(bf16r(y0) + bflo(b2p)), yb = bf16r(bf16r(y1) + bfhi(b2p));
      reinterpret_cast<float*>(lds + L_G)[2 * lane] = bf16r(ya / (1.0f + __expf(-ya)));
      reinterpret_cast<float*>(lds + L_G)[2 * lane + 1] = bf16r(yb / (1.0f + __expf(-yb)));
    }
  }
  wg_barrier();
  if (ROLE != 0) {
    tag.lo = (u32)__builtin_amdgcn_readfirstlane(flags[6]);
    tag.hi = (u32)__builtin_amdgcn_readfirstlane(flags[7]);
  }
  if (v == 0) STAMP(7);

  if (tid < 256) {
    const int g = tid >> 6, d2 = tid & 63;
    const float* mrg = reinterpret_cast<const float*>(lds + L_MRG) + g * 132;
    float M = mrg[0];
#pragma unroll
    for (int u = 1; u < 8; ++u) M = fmaxf(M, mrg[u * 4 * 132]);
    float L = 0.f, o0 = 0.f, o1 = 0.f;
#pragma unroll
    for (int u = 0; u < 8; ++u) {
      const float wt = __builtin_amdgcn_exp2f((mrg[u * 4 * 132] - M) * LOG2E);
      L = fmaf(wt, mrg[u * 4 * 132 + 1], L);
      o0 = fmaf(wt, mrg[u * 4 * 132 + 4 + 2 * d2], o0);
      o1 = fmaf(wt, mrg[u * 4 * 132 + 5 + 2 * d2], o1);
    }
    const u32 pg = G_P + ((ab * HQ + ahkv * 4 + g) * 4 + split) * 66;
    put_granule(rs, pg + d2, __float_as_uint(o0), __float_as_uint(o1), tag);
    if (d2 == 0) put_granule(rs, pg + 64, __float_as_uint(M), __float_as_uint(L), tag);
  }

  s16x8 wof[4];
  if (ROLE <= 1) {
#pragma unroll
    for (int s = 0; s < 4; ++s)
#pragma unroll
      for (int jj = 0; jj < 8; ++jj)
        wof[s][jj] = *reinterpret_cast<const short*>(lds + L_WO + (32 * s + 8 * r + jj) * WROW + (16 * v + c) * 2);
  }
  u32 xp = 0;
  if (ROLE == 2 && hi < 8 && v == 4) xp = *reinterpret_cast<const u32*>(a.x + hi * DM + 64 * cj + 2 * (lane & 31));
  {
    const u32 pg = G_P + (v * HQ + hi) * 4 * 66;
    probe_wait(rs, pg + (lane & 3) * 66 + 64, lane < 4, tag);
    u32x4 go[4], gm[4];
    for (u32 spins = 0; spins < SPIN_CAP; ++spins) {
      asm volatile("" ::: "memory");
      bool ok = true;
#pragma unroll
      for (int s = 0; s < 4; ++s) {
        go[s] = get_granule(rs, pg + s * 66 + lane);
        gm[s] = get_granule(rs, pg + s * 66 + 64);
        ok &= tag_ok(go[s], tag) & tag_ok(gm[s], tag);
      }
      if (__all(ok)) break;
      __builtin_amdgcn_s_sleep(2);
    }
    float M = __uint_as_float(gm[0][0]);
#pragma unroll
    for (int s = 1; s < 4; ++s) M = fmaxf(M, __uint_as_float(gm[s][0]));
    float L = 0.f, a0 = 0.f, a1 = 0.f;
#pragma unroll
    for (int s = 0; s < 4; ++s) {
      const float wt = __builtin_amdgcn_exp2f((__uint_as_float(gm[s][0]) - M) * LOG2E);
      L = fmaf(wt, __uint_as_float(gm[s][1]), L);
      a0 = fmaf(wt, __uint_as_float(go[s][0]), a0);
      a1 = fmaf(wt, __uint_as_float(go[s][1]), a1);
    }
    const float inv = 1.0f / L;
    reinterpret_cast<u32*>(lds + L_O)[v * 64 + lane] = pack2(a0 * inv, a1 * inv);
  }
  wg_barrier();
  if (v == 0) STAMP(8);
  if (ROLE <= 1) {
    const int ct = v;
    f32x4 acc4 = {0.f, 0.f, 0.f, 0.f};
#pragma unroll
    for (int s = 0; s < 4; ++s) {
      const u32x4 af = *reinterpret_cast<const u32x4*>(lds + L_O + c * 256 + (32 * s + 8 * r) * 2);
      acc4 = __builtin_amdgcn_mfma_f32_16x16x32_bf16(__builtin_bit_cast(s16x8, af), wof[s], acc4, 0, 0, 0);
    }
#pragma unroll
    for (int k = 0; k < 4; ++k) {
      const float nb = dpp<XOR1>(acc4[k]);
      if (lane < 32 && (c & 1) == 0)
        put_granule(rs, G_S + ((cj * NB + (4 * r + k)) * 32 + hi) * 32 + 8 * ct + (c >> 1), __float_as_uint(acc4[k]), __float_as_uint(nb), tag);
    }
  }
  if (ROLE == 2 && hi < 8 && v == 4) {
    const u32 sg = G_S + (cj * NB + hi) * 32 * 32;
    float s0 = 0.f, s1 = 0.f;
    for (u32 spins = 0; spins < SPIN_CAP; ++spins) {
      asm volatile("" ::: "memory");
      bool ok = true; s0 = 0.f; s1 = 0.f;
#pragma unroll
      for (int i = 0; i < 16; ++i) { const u32x4 g = get_granule(rs, sg + lane + 64 * i); s0 += __uint_as_float(g[0]); s1 += __uint_as_float(g[1]); ok &= tag_ok(g, tag); }
      if (__all(ok)) break;
      __builtin_amdgcn_s_sleep(2);
    }
    {
      auto t0 = __builtin_amdgcn_permlane32_swap(__float_as_uint(s0), __float_as_uint(s0), false, false);
      s0 = __uint_as_float(t0[0]) + __uint_as_float(t0[1]);
      auto t1 = __builtin_amdgcn_permlane32_swap(__float_as_uint(s1), __float_as_uint(s1), false, false);
      s1 = __uint_as_float(t1[0]) + __uint_as_float(t1[1]);
    }
    if (lane < 32) {
      const float g0 = reinterpret_cast<const float*>(lds + L_G)[2 * lane], g1 = reinterpret_cast<const float*>(lds + L_G)[2 * lane + 1];
      const float r0 = bflo(xp) + bf16r(bf16r(s0) * g0), r1 = bfhi(xp) + bf16r(bf16r(s1) * g1);
      *reinterpret_cast<u32*>(a.out + hi * DM + 64 * cj + 2 * lane) = pack2(r0, r1);
    }
    if (w == 0 && lane == 0)
      __hip_atomic_store(a.ws, ((u64)tag.hi << 32) | tag.lo, __ATOMIC_RELAXED, __HIP_MEMORY_SCOPE_AGENT);
    STAMP(9);
  }
}

__global__ void __launch_bounds__(NTHR, 2) fused_decode_kernel(Params a) {
  __shared__ __attribute__((aligned(16))) unsigned char lds[L_END];
  const int v = __builtin_amdgcn_readfirstlane(threadIdx.x >> 6);
  if (v == 0) body<0>(a, lds, 0);
  else if (v < 4) body<1>(a, lds, v);
  else body<2>(a, lds, v);
}

extern "C" void kernel_launch(void* const* d_in, const int* in_sizes, int n_in,
                              void* d_out, int out_size, void* d_ws, size_t ws_size,
                              hipStream_t stream) {
  Params p;
  p.x = (const u16*)d_in[0];
  p.kc = (const u16*)d_in[1];
  p.vc = (const u16*)d_in[2];
  p.wq = (const u16*)d_in[3];
  p.wo = (const u16*)d_in[4];
  p.w1 = (const u16*)d_in[6];
  p.b1 = (const u16*)d_in[7];
  p.w2 = (const u16*)d_in[8];
  p.b2 = (const u16*)d_in[9];
  p.out = (u16*)d_out;
  p.ws = (u64*)d_ws;
  hipLaunchKernelGGL(fused_decode_kernel, dim3(NWG), dim3(NTHR), 0, stream, p);
}
```

```cpp
#include <hip/hip_runtime.h>
#include <hip/hip_bf16.h>
#include <cstdint>

typedef unsigned int u32;
typedef unsigned long long u64;
typedef unsigned short u16;
using u32x4 = __attribute__((ext_vector_type(4))) u32;
using f32x4 = __attribute__((ext_vector_type(4))) float;
using f32x2 = __attribute__((ext_vector_type(2))) float;
using s16x8 = __attribute__((ext_vector_type(8))) short;
using bf16x2 = __attribute__((ext_vector_type(2))) __bf16;

constexpr int NB = 8, DM = 512, HQ = 32, HKV = 8, SK = 4096, DH = 128, FF = 1792;
constexpr int NWG = 256, NTHR = 512;
constexpr u32 SPIN_CAP = 1u << 20;
constexpr float LOG2E = 1.4426950408889634f;

constexpr u32 G_Q = 16;
constexpr u32 G_H = G_Q + NB * 1024;
constexpr u32 G_P = G_H + NB * 448;
constexpr u32 G_S = G_P + NB * HQ * 4 * 66;
constexpr u32 G_Y = G_S + 8 * NB * 32 * 32;
constexpr u32 G_END = G_Y + 8 * NB * 32 * 32;

__device__ __forceinline__ float bf16r(float x) { return (float)(__bf16)x; }
__device__ __forceinline__ u32 pack2(float lo, float hi) {
  bf16x2 v = {(__bf16)lo, (__bf16)hi};
  return __builtin_bit_cast(u32, v);
}
__device__ __forceinline__ float bflo(u32 x) { return __builtin_bit_cast(float, x << 16); }
__device__ __forceinline__ float bfhi(u32 x) { return __builtin_bit_cast(float, x & 0xffff0000u); }
__device__ __forceinline__ float bfu(u16 x) { return __builtin_bit_cast(float, (u32)x << 16); }
__device__ __forceinline__ float dot2(u32 k, u32 q, float acc) {
  return __builtin_amdgcn_fdot2_f32_bf16(__builtin_bit_cast(bf16x2, k), __builtin_bit_cast(bf16x2, q), acc, false);
}
template <int CTRL> __device__ __forceinline__ float dpp(float x) {
  return __builtin_bit_cast(float, __builtin_amdgcn_mov_dpp(__builtin_bit_cast(int, x), CTRL, 0xf, 0xf, true));
}
constexpr int XOR1 = 0xB1, XOR2 = 0x4E, XOR7 = 0x141, XOR8 = 0x128, ROR4 = 0x124;
__device__ __forceinline__ float xrow16_max(float x) {
  auto s = __builtin_amdgcn_permlane16_swap(__float_as_uint(x), __float_as_uint(x), false, false);
  x = fmaxf(__uint_as_float(s[0]), __uint_as_float(s[1]));
  auto t = __builtin_amdgcn_permlane32_swap(__float_as_uint(x), __float_as_uint(x), false, false);
  return fmaxf(__uint_as_float(t[0]), __uint_as_float(t[1]));
}
__device__ __forceinline__ float xrow16_sum(float x) {
  auto s = __builtin_amdgcn_permlane16_swap(__float_as_uint(x), __float_as_uint(x), false, false);
  x = __uint_as_float(s[0]) + __uint_as_float(s[1]);
  auto t = __builtin_amdgcn_permlane32_swap(__float_as_uint(x), __float_as_uint(x), false, false);
  return __uint_as_float(t[0]) + __uint_as_float(t[1]);
}
typedef __amdgpu_buffer_rsrc_t rsrc_t;
struct Tag { u32 lo, hi; };
__device__ __forceinline__ void put_granule(rsrc_t rs, u32 g, u32 v0, u32 v1, Tag t) {
  __builtin_amdgcn_raw_buffer_store_b128(u32x4{v0, v1, t.lo, t.hi}, rs, (int)(g * 16u), 0, 16);
}
__device__ __forceinline__ u32x4 get_granule(rsrc_t rs, u32 g) {
  return __builtin_amdgcn_raw_buffer_load_b128(rs, (int)(g * 16u), 0, 16);
}
__device__ __forceinline__ bool tag_ok(u32x4 g, Tag t) { return (g[2] == t.lo) & (g[3] == t.hi); }
__device__ __forceinline__ void wg_barrier() {
  asm volatile("s_waitcnt lgkmcnt(0)" ::: "memory");
  __builtin_amdgcn_s_barrier();
  asm volatile("" ::: "memory");
}
__device__ __forceinline__ u32x4 ld_nt(const u16* p) {
  return __builtin_nontemporal_load(reinterpret_cast<const u32x4*>(p));
}
__device__ __forceinline__ u32 lds_flag_load(u32* f) {
  return (u32)__builtin_amdgcn_readfirstlane(__hip_atomic_load(f, __ATOMIC_RELAXED, __HIP_MEMORY_SCOPE_WORKGROUP));
}
__device__ __forceinline__ void lds_wait_ge(u32* f, u32 want) {
  for (u32 spins = 0; spins < SPIN_CAP * 8u; ++spins) {
    if (lds_flag_load(f) >= want) break;
    __builtin_amdgcn_s_sleep(1);
  }
  asm volatile("" ::: "memory");
}
__device__ __forceinline__ void probe_wait(rsrc_t rs, u32 g, bool active, Tag t) {
  for (u32 spins = 0; spins < SPIN_CAP; ++spins) {
    asm volatile("" ::: "memory");
    bool ok = true;
    if (active) ok = tag_ok(get_granule(rs, g), t);
    if (__all(ok)) break;
    __builtin_amdgcn_s_sleep(2);
  }
}

struct Params {
  const u16* x; const u16* kc; const u16* vc; const u16* wq; const u16* wo;
  const u16* w1; const u16* b1; const u16* w2; const u16* b2;
  u16* out; u64* ws;
};

constexpr int WROW = 144;
constexpr int L_WO = 0;
constexpr int L_W2 = L_WO + 128 * WROW;
constexpr int L_SVC = L_W2 + 64 * WROW;
constexpr int L_Q = L_SVC + 4 * 2 * 8 * 16 * 4;
constexpr int L_H = L_Q + 1024;
constexpr int L_O = L_H + 16 * 32 * 4;
constexpr int L_G = L_O + 16 * 64 * 4;
constexpr int L_FLAG = L_G + 256;
constexpr int L_P = L_FLAG + 32;
constexpr int L_MRG = L_P + 8 * 144 * 4 * 4;
constexpr int L_WQS = L_MRG + 8 * 4 * 132 * 4;
constexpr int L_W1S = L_WQS + 4 * 4352;
constexpr int L_XN = L_W1S + 4 * 2304;
constexpr int L_END = L_XN + 64;

template <int ROLE>
__device__ __forceinline__ void body(const Params& a, unsigned char* lds, const int v) {
  constexpr int NG = (ROLE == 0) ? 1 : 9;
  const int w = blockIdx.x;
  const int tid = threadIdx.x, lane = tid & 63;
  const int r = lane >> 4, c = lane & 15;
  const int bh = w >> 2, split = w & 3;
  const int ab = bh >> 3, ahkv = bh & 7;
  const int hi = w >> 3, cj = w & 7;
  const int qb = (w & 7) * 32 + (w >> 3);
  const bool h_on = (qb < 224);
  const rsrc_t rs = __builtin_amdgcn_make_buffer_rsrc((void*)a.ws, 0, (int)(G_END * 16u), 0x00020000);
  Tag tag = {0u, 0u};
  u64 epoch_raw = 0;
  if (ROLE == 0) epoch_raw = __hip_atomic_load(a.ws, __ATOMIC_RELAXED, __HIP_MEMORY_SCOPE_AGENT);
  u32* flags = reinterpret_cast<u32*>(lds + L_FLAG);

  if (ROLE == 0) {
    if (lane < 6) flags[lane] = 0u;
  }
  wg_barrier();
  __builtin_amdgcn_s_setprio(3);

  const int key0 = (ROLE == 0) ? 0 : 16 + 144 * (v - 1);
  const size_t kvbase = ((size_t)bh * SK + split * 1024 + key0) * DH + (size_t)r * DH + 8 * c;
  const size_t kfbase = ((size_t)bh * SK + split * 1024 + key0) * DH + (size_t)c * DH + 8 * r;
  constexpr int VPG = (ROLE == 0) ? 0 : 3;
  u32x4 kv[4 * NG];
  u32x4 vpre[4 * VPG + 1];
  u32 b1v = 0;

  if (ROLE <= 1) {
    u32x4 xs[2], xa[4];
#pragma unroll
    for (int i = 0; i < 2; ++i) xs[i] = ld_nt(a.x + (2 * v + i) * DM + 8 * lane);
#pragma unroll
    for (int s = 0; s < 4; ++s) xa[s] = ld_nt(a.x + (c & 7) * DM + 128 * v + 32 * s + 8 * r);
    u32x4 wqr[4], w1r[2];
#pragma unroll
    for (int u = 0; u < 4; ++u)
      wqr[u] = ld_nt(a.wq + (size_t)(128 * v + 32 * u + (lane >> 1)) * 4096 + 16 * qb + 8 * (lane & 1));
#pragma unroll
    for (int u = 0; u < 2; ++u)
      w1r[u] = ld_nt(a.w1 + (size_t)(128 * v + 64 * u + lane) * FF + 8 * (h_on ? qb : 0));
    if (ROLE == 0) b1v = a.b1[8 * (h_on ? qb : 0) + (lane & 7)];
    __builtin_amdgcn_sched_barrier(0);
    if (lane == 0) __hip_atomic_fetch_add(flags + 3, 1u, __ATOMIC_RELAXED, __HIP_MEMORY_SCOPE_WORKGROUP);
    float* stat = reinterpret_cast<float*>(lds + L_XN);
#pragma unroll
    for (int i = 0; i < 2; ++i) {
      float s = 0.f;
#pragma unroll
      for (int k = 0; k < 4; ++k) s += bflo(xs[i][k]) + bfhi(xs[i][k]);
      s += dpp<XOR1>(s); s += dpp<XOR2>(s); s += dpp<XOR7>(s); s += dpp<XOR8>(s); s = xrow16_sum(s);
      const float mean = s * (1.0f / DM);
      float q2 = 0.f;
#pragma unroll
      for (int k = 0; k < 4; ++k) {
        const float d0 = bflo(xs[i][k]) - mean, d1 = bfhi(xs[i][k]) - mean;
        q2 = fmaf(d0, d0, q2); q2 = fmaf(d1, d1, q2);
      }
      q2 += dpp<XOR1>(q2); q2 += dpp<XOR2>(q2); q2 += dpp<XOR7>(q2); q2 += dpp<XOR8>(q2); q2 = xrow16_sum(q2);
      const float var_bf = bf16r(q2 * (1.0f / DM));
      if (lane == 0) { stat[4 * v + 2 * i] = bf16r(mean); stat[4 * v + 2 * i + 1] = bf16r(sqrtf(bf16r(var_bf + 1e-5f))); }
    }
    asm volatile("s_waitcnt lgkmcnt(0)" ::: "memory");
    if (lane == 0) __hip_atomic_fetch_add(flags + 4, 1u, __ATOMIC_RELAXED, __HIP_MEMORY_SCOPE_WORKGROUP);
    unsigned char* wqs = lds + L_WQS + v * 4352;
    unsigned char* w1s = lds + L_W1S + v * 2304;
#pragma unroll
    for (int u = 0; u < 4; ++u) {
      const int rr = 32 * u + (lane >> 1);
      *reinterpret_cast<u32x4*>(wqs + rr * 32 + (rr >> 3) * 16 + 16 * (lane & 1)) = wqr[u];
    }
#pragma unroll
    for (int u = 0; u < 2; ++u) {
      const int rr = 64 * u + lane;
      *reinterpret_cast<u32x4*>(w1s + rr * 16 + (rr >> 3) * 16) = w1r[u];
    }
    lds_wait_ge(flags + 4, 4u);
    const float mean_bf = stat[2 * (c & 7)], sd_bf = stat[2 * (c & 7) + 1];
    asm volatile("s_waitcnt lgkmcnt(0)" ::: "memory");
    f32x4 accq = {0.f, 0.f, 0.f, 0.f}, acch = {0.f, 0.f, 0.f, 0.f};
#pragma unroll
    for (int s = 0; s < 4; ++s) {
      u32x4 af;
#pragma unroll
      for (int k = 0; k < 4; ++k)
        af[k] = (c < 8) ? pack2(bf16r(bflo(xa[s][k]) - mean_bf) / sd_bf, bf16r(bfhi(xa[s][k]) - mean_bf) / sd_bf) : 0u;
      const s16x8 afr = __builtin_bit_cast(s16x8, af);
      s16x8 bq, bws;
#pragma unroll
      for (int jj = 0; jj < 8; ++jj) {
        const int k = 32 * s + 8 * r + jj;
        bq[jj] = *reinterpret_cast<const short*>(wqs + k * 32 + (k >> 3) * 16 + c * 2);
        bws[jj] = *reinterpret_cast<const short*>(w1s + k * 16 + (k >> 3) * 16 + (c & 7) * 2);
      }
      if (c >= 8) bws = s16x8{0, 0, 0, 0, 0, 0, 0, 0};
      accq = __builtin_amdgcn_mfma_f32_16x16x32_bf16(afr, bq, accq, 0, 0, 0);
      acch = __builtin_amdgcn_mfma_f32_16x16x32_bf16(afr, bws, acch, 0, 0, 0);
    }
    float* svc = reinterpret_cast<float*>(lds + L_SVC) + v * 256;
    if (lane < 32) {
#pragma unroll
      for (int k = 0; k < 4; ++k) {
        svc[(4 * r + k) * 16 + c] = accq[k];
        svc[128 + (4 * r + k) * 16 + c] = acch[k];
      }
    }
    if (ROLE == 1) {
      asm volatile("s_waitcnt lgkmcnt(0)" ::: "memory");
      if (lane == 0) __hip_atomic_fetch_add(flags + 1, 1u, __ATOMIC_RELAXED, __HIP_MEMORY_SCOPE_WORKGROUP);
    }
  }

  if (ROLE == 2) {
    const int t4 = tid - 256;
    lds_wait_ge(flags + 3, 4u);
    u32x4 st[6];
#pragma unroll
    for (int u = 0; u < 4; ++u) {
      const int e = t4 + 256 * u, row = e >> 3, seg = e & 7;
      st[u] = ld_nt(a.wo + (size_t)(128 * hi + row) * DM + 64 * cj + 8 * seg);
    }
#pragma unroll
    for (int u = 0; u < 2; ++u) {
      const int e = t4 + 256 * u, row = e >> 3, seg = e & 7;
      st[4 + u] = ld_nt(a.w2 + (size_t)(56 * hi + (row < 56 ? row : 0)) * DM + 64 * cj + 8 * seg);
    }
#pragma unroll
    for (int i = 0; i < 4 * NG; ++i) kv[i] = ld_nt(a.kc + kfbase + (size_t)(i >> 2) * 16 * DH + (i & 3) * 32);
#pragma unroll
    for (int i = 0; i < 4 * VPG; ++i) vpre[i] = ld_nt(a.vc + kvbase + (size_t)i * 4 * DH);
    __builtin_amdgcn_sched_barrier(0);
#pragma unroll
    for (int u = 0; u < 4; ++u) {
      const int e = t4 + 256 * u, row = e >> 3, seg = e & 7;
      *reinterpret_cast<u32x4*>(lds + L_WO + row * WROW + 16 * seg) = st[u];
    }
#pragma unroll
    for (int u = 0; u < 2; ++u) {
      const int e = t4 + 256 * u, row = e >> 3, seg = e & 7;
      *reinterpret_cast<u32x4*>(lds + L_W2 + row * WROW + 16 * seg) = row < 56 ? st[4 + u] : u32x4{0u, 0u, 0u, 0u};
    }
    reinterpret_cast<u32*>(lds + L_H)[256 + t4] = 0u;
    reinterpret_cast<u32*>(lds + L_O)[512 + t4] = 0u;
    reinterpret_cast<u32*>(lds + L_O)[768 + t4] = 0u;
    asm volatile("s_waitcnt lgkmcnt(0)" ::: "memory");
    if (lane == 0) __hip_atomic_fetch_add(flags + 2, 1u, __ATOMIC_RELAXED, __HIP_MEMORY_SCOPE_WORKGROUP);
  }
  if (ROLE == 1) {
#pragma unroll
    for (int i = 0; i < 4 * NG; ++i) kv[i] = ld_nt(a.kc + kfbase + (size_t)(i >> 2) * 16 * DH + (i & 3) * 32);
#pragma unroll
    for (int i = 0; i < 4 * VPG; ++i) vpre[i] = ld_nt(a.vc + kvbase + (size_t)i * 4 * DH);
    __builtin_amdgcn_sched_barrier(0);
  }

  if (ROLE == 0) {
    lds_wait_ge(flags + 1, 3u);
    {
      const u64 e = epoch_raw + 1ull;
      tag.lo = (u32)__builtin_amdgcn_readfirstlane((u32)e);
      tag.hi = (u32)__builtin_amdgcn_readfirstlane((u32)(e >> 32));
      if (lane == 0) { flags[6] = tag.lo; flags[7] = tag.hi; }
    }
    const float* svc = reinterpret_cast<const float*>(lds + L_SVC);
    if (lane < 32) {
      const int b = lane >> 2, cq = lane & 3;
      float sv[4];
#pragma unroll
      for (int e = 0; e < 4; ++e) {
        float s0 = 0.f;
#pragma unroll
        for (int u = 0; u < 4; ++u) s0 += svc[u * 256 + b * 16 + 4 * cq + e];
        sv[e] = s0;
      }
      put_granule(rs, G_Q + b * 1024 + 4 * qb + cq, pack2(sv[0], sv[1]), pack2(sv[2], sv[3]), tag);
    }
    {
      const int b = lane >> 3, col = lane & 7;
      float s0 = 0.f;
#pragma unroll
      for (int u = 0; u < 4; ++u) s0 += svc[u * 256 + 128 + b * 16 + col];
      const float t = bf16r(bf16r(s0) + bfu((u16)b1v));
      const float inner = 0.7978845608028654f * (t + 0.044715f * t * t * t);
      const float hv = 0.5f * t * (1.0f + tanhf(inner));
      const float h1 = dpp<XOR1>(hv);
      const u32 pr = (lane & 1) ? pack2(h1, hv) : pack2(hv, h1);
      const u32 pr2 = __builtin_bit_cast(u32, dpp<XOR2>(__builtin_bit_cast(float, pr)));
      if (h_on && (lane & 3) == 0) put_granule(rs, G_H + b * 448 + 2 * qb + (col >> 2), pr, pr2, tag);
    }
    u32x4 gq[2], gh[2];
    for (u32 spins = 0; spins < SPIN_CAP; ++spins) {
      asm volatile("" ::: "memory");
      bool ok = true;
#pragma unroll
      for (int k = 0; k < 2; ++k) { gq[k] = get_granule(rs, G_Q + ab * 1024 + ahkv * 128 + 64 * k + lane); ok &= tag_ok(gq[k], tag); }
      if (__all(ok)) break;
      __builtin_amdgcn_s_sleep(1);
    }
    u64* qs = reinterpret_cast<u64*>(lds + L_Q);
#pragma unroll
    for (int k = 0; k < 2; ++k) qs[64 * k + lane] = ((u64)gq[k][1] << 32) | gq[k][0];
    asm volatile("s_waitcnt lgkmcnt(0)" ::: "memory");
    if (lane == 0) __hip_atomic_store(flags, 1u, __ATOMIC_RELAXED, __HIP_MEMORY_SCOPE_WORKGROUP);
#pragma unroll
    for (int i = 0; i < 4 * NG; ++i) kv[i] = ld_nt(a.kc + kfbase + (size_t)(i >> 2) * 16 * DH + (i & 3) * 32);
    u32 hg[2]; int hidx[2];
#pragma unroll
    for (int k = 0; k < 2; ++k) {
      const int idx = min(lane + 64 * k, 111);
      const int b = idx / 14, t = idx % 14;
      hg[k] = G_H + b * 448 + 14 * hi + t;
      hidx[k] = b * 16 + t;
    }
    for (u32 spins = 0; spins < SPIN_CAP; ++spins) {
      asm volatile("" ::: "memory");
      bool ok = true;
#pragma unroll
      for (int k = 0; k < 2; ++k) { gh[k] = get_granule(rs, hg[k]); ok &= tag_ok(gh[k], tag); }
      if (__all(ok)) break;
      __builtin_amdgcn_s_sleep(1);
    }
    u64* hs = reinterpret_cast<u64*>(lds + L_H);
#pragma unroll
    for (int k = 0; k < 2; ++k) if (lane + 64 * k < 112) hs[hidx[k]] = ((u64)gh[k][1] << 32) | gh[k][0];
    if (lane < 16) hs[(lane >> 1) * 16 + 14 + (lane & 1)] = 0ull;
    lds_wait_ge(flags + 2, 4u);
    asm volatile("s_waitcnt lgkmcnt(0)" ::: "memory");
#pragma unroll
    for (int ct = 0; ct < 4; ++ct) {
      f32x4 acc = {0.f, 0.f, 0.f, 0.f};
#pragma unroll
      for (int s = 0; s < 2; ++s) {
        const u32x4 af = *reinterpret_cast<const u32x4*>(lds + L_H + c * 128 + (32 * s + 8 * r) * 2);
        s16x8 bf;
#pragma unroll
        for (int jj = 0; jj < 8; ++jj)
          bf[jj] = *reinterpret_cast<const short*>(lds + L_W2 + (32 * s + 8 * r + jj) * WROW + (16 * ct + c) * 2);
        acc = __builtin_amdgcn_mfma_f32_16x16x32_bf16(__builtin_bit_cast(s16x8, af), bf, acc, 0, 0, 0);
      }
#pragma unroll
      for (int k = 0; k < 4; ++k) {
        const float nb = dpp<XOR1>(acc[k]);
        if (lane < 32 && (c & 1) == 0)
          put_granule(rs, G_Y + ((cj * NB + (4 * r + k)) * 32 + hi) * 32 + 8 * ct + (c >> 1), __float_as_uint(acc[k]), __float_as_uint(nb), tag);
      }
    }
  } else {
    __builtin_amdgcn_s_setprio(0);
    lds_wait_ge(flags, 1u);
  }

  s16x8 qf[4];
#pragma unroll
  for (int s = 0; s < 4; ++s) {
    u32x4 t = *reinterpret_cast<const u32x4*>(lds + L_Q + (c & 3) * 256 + 64 * s + 16 * r);
    if (c >= 4) t = u32x4{0u, 0u, 0u, 0u};
    qf[s] = __builtin_bit_cast(s16x8, t);
  }
  float sc[NG][4];
#pragma unroll
  for (int grp = 0; grp < NG; ++grp) {
    f32x4 d = {0.f, 0.f, 0.f, 0.f};
#pragma unroll
    for (int s = 0; s < 4; ++s) d = __builtin_amdgcn_mfma_f32_16x16x32_bf16(__builtin_bit_cast(s16x8, kv[4 * grp + s]), qf[s], d, 0, 0, 0);
#pragma unroll
    for (int k = 0; k < 4; ++k) sc[grp][k] = bf16r(bf16r(d[k]) * 0.08838834764831845f);
  }
  float m = sc[0][0];
#pragma unroll
  for (int grp = 0; grp < NG; ++grp)
#pragma unroll
    for (int k = 0; k < 4; ++k) m = fmaxf(m, sc[grp][k]);
  m = xrow16_max(m);
  float l = 0.f;
  {
    float* pl = reinterpret_cast<float*>(lds + L_P) + v * 576;
#pragma unroll
    for (int grp = 0; grp < NG; ++grp)
#pragma unroll
      for (int k = 0; k < 4; ++k) {
        const float p = __builtin_amdgcn_exp2f((sc[grp][k] - m) * LOG2E);
        l += p;
        if (c < 4) pl[(16 * grp + 4 * r + k) * 4 + c] = p;
      }
  }
  l = xrow16_sum(l);
  asm volatile("s_waitcnt lgkmcnt(0)" ::: "memory");

  constexpr int VAH = 3;
  f32x2 acc2[16];
#pragma unroll
  for (int k = 0; k < 16; ++k) acc2[k] = f32x2{0.f, 0.f};
  {
    const f32x4* pl4 = reinterpret_cast<const f32x4*>(lds + L_P) + v * 144;
#pragma unroll
    for (int grp = 0; grp < NG; ++grp) {
#pragma unroll
      for (int gg = (grp == 0 ? VPG : VPG + VAH - 1 + grp); gg <= VPG + VAH - 1 + grp; ++gg) {
        if (gg < NG) {
#pragma unroll
          for (int t = 0; t < 4; ++t) kv[4 * (gg - VPG) + t] = ld_nt(a.vc + kvbase + (size_t)(4 * gg + t) * 4 * DH);
        }
      }
      __builtin_amdgcn_sched_barrier(0);
#pragma unroll
      for (int t = 0; t < 4; ++t) {
        const int i = 4 * grp + t;
        const f32x4 p = pl4[4 * i + r];
#pragma unroll
        for (int k = 0; k < 4; ++k) {
          const u32 vv = (i < 4 * VPG) ? vpre[i < 4 * VPG ? i : 0][k] : kv[i >= 4 * VPG ? i - 4 * VPG : 0][k];
          const f32x2 vf = {bflo(vv), bfhi(vv)};
#pragma unroll
          for (int g = 0; g < 4; ++g) acc2[g * 4 + k] = __builtin_elementwise_fma(f32x2{p[g], p[g]}, vf, acc2[g * 4 + k]);
        }
      }
      __builtin_amdgcn_sched_barrier(0);
    }
  }
  float acc[32];
#pragma unroll
  for (int k = 0; k < 32; ++k) acc[k] = xrow16_sum(acc2[k >> 1][k & 1]);
  {
    float* mrg = reinterpret_cast<float*>(lds + L_MRG) + v * 4 * 132;
    if (r == 0) {
#pragma unroll
      for (int g = 0; g < 4; ++g) {
        *reinterpret_cast<f32x4*>(mrg + g * 132 + 4 + 8 * c) = f32x4{acc[g * 8], acc[g * 8 + 1], acc[g * 8 + 2], acc[g * 8 + 3]};
        *reinterpret_cast<f32x4*>(mrg + g * 132 + 4 + 8 * c + 4) = f32x4{acc[g * 8 + 4], acc[g * 8 + 5], acc[g * 8 + 6], acc[g * 8 + 7]};
      }
      if (c < 4) { mrg[c * 132] = m; mrg[c * 132 + 1] = l; }
    }
  }
  if (ROLE == 0 && hi < 8) {
    const u32 yg = G_Y + (cj * NB + hi) * 32 * 32 + (lane & 31) + (lane >> 5) * 32;
    float y0 = 0.f, y1 = 0.f;
    for (u32 spins = 0; spins < SPIN_CAP; ++spins) {
      asm volatile("" ::: "memory");
      bool ok = true; y0 = 0.f; y1 = 0.f;
#pragma unroll
      for (int i = 0; i < 16; ++i) { const u32x4 g = get_granule(rs, yg + 64 * i); y0 += __uint_as_float(g[0]); y1 += __uint_as_float(g[1]); ok &= tag_ok(g, tag); }
      if (__all(ok)) break;
      __builtin_amdgcn_s_sleep(8);
    }
    {
      auto t0 = __builtin_amdgcn_permlane32_swap(__float_as_uint(y0), __float_as_uint(y0), false, false);
      y0 = __uint_as_float(t0[0]) + __uint_as_float(t0[1]);
      auto t1 = __builtin_amdgcn_permlane32_swap(__float_as_uint(y1), __float_as_uint(y1), false, false);
      y1 = __uint_as_float(t1[0]) + __uint_as_float(t1[1]);
    }
    if (lane < 32) {
      const u32 b2p = *reinterpret_cast<const u32*>(a.b2 + 64 * cj + 2 * lane);
      const float ya = bf16r(bf16r(y0) + bflo(b2p)), yb = bf16r(bf16r(y1) + bfhi(b2p));
      reinterpret_cast<float*>(lds + L_G)[2 * lane] = bf16r(ya / (1.0f + __expf(-ya)));
      reinterpret_cast<float*>(lds + L_G)[2 * lane + 1] = bf16r(yb / (1.0f + __expf(-yb)));
    }
  }
  wg_barrier();
  if (ROLE != 0) {
    tag.lo = (u32)__builtin_amdgcn_readfirstlane(flags[6]);
    tag.hi = (u32)__builtin_amdgcn_readfirstlane(flags[7]);
  }

  if (tid < 256) {
    const int g = tid >> 6, d2 = tid & 63;
    const float* mrg = reinterpret_cast<const float*>(lds + L_MRG) + g * 132;
    float M = mrg[0];
#pragma unroll
    for (int u = 1; u < 8; ++u) M = fmaxf(M, mrg[u * 4 * 132]);
    float L = 0.f, o0 = 0.f, o1 = 0.f;
#pragma unroll
    for (int u = 0; u < 8; ++u) {
      const float wt = __builtin_amdgcn_exp2f((mrg[u * 4 * 132] - M) * LOG2E);
      L = fmaf(wt, mrg[u * 4 * 132 + 1], L);
      o0 = fmaf(wt, mrg[u * 4 * 132 + 4 + 2 * d2], o0);
      o1 = fmaf(wt, mrg[u * 4 * 132 + 5 + 2 * d2], o1);
    }
    const u32 pg = G_P + ((ab * HQ + ahkv * 4 + g) * 4 + split) * 66;
    put_granule(rs, pg + d2, __float_as_uint(o0), __float_as_uint(o1), tag);
    if (d2 == 0) put_granule(rs, pg + 64, __float_as_uint(M), __float_as_uint(L), tag);
  }

  s16x8 wof[4];
  if (ROLE <= 1) {
#pragma unroll
    for (int s = 0; s < 4; ++s)
#pragma unroll
      for (int jj = 0; jj < 8; ++jj)
        wof[s][jj] = *reinterpret_cast<const short*>(lds + L_WO + (32 * s + 8 * r + jj) * WROW + (16 * v + c) * 2);
  }
  u32 xp = 0;
  if (ROLE == 2 && hi < 8 && v == 4) xp = *reinterpret_cast<const u32*>(a.x + hi * DM + 64 * cj + 2 * (lane & 31));
  {
    const u32 pg = G_P + (v * HQ + hi) * 4 * 66;
    probe_wait(rs, pg + (lane & 3) * 66 + 64, lane < 4, tag);
    u32x4 go[4], gm[4];
    for (u32 spins = 0; spins < SPIN_CAP; ++spins) {
      asm volatile("" ::: "memory");
      bool ok = true;
#pragma unroll
      for (int s = 0; s < 4; ++s) {
        go[s] = get_granule(rs, pg + s * 66 + lane);
        gm[s] = get_granule(rs, pg + s * 66 + 64);
        ok &= tag_ok(go[s], tag) & tag_ok(gm[s], tag);
      }
      if (__all(ok)) break;
      __builtin_amdgcn_s_sleep(2);
    }
    float M = __uint_as_float(gm[0][0]);
#pragma unroll
    for (int s = 1; s < 4; ++s) M = fmaxf(M, __uint_as_float(gm[s][0]));
    float L = 0.f, a0 = 0.f, a1 = 0.f;
#pragma unroll
    for (int s = 0; s < 4; ++s) {
      const float wt = __builtin_amdgcn_exp2f((__uint_as_float(gm[s][0]) - M) * LOG2E);
      L = fmaf(wt, __uint_as_float(gm[s][1]), L);
      a0 = fmaf(wt, __uint_as_float(go[s][0]), a0);
      a1 = fmaf(wt, __uint_as_float(go[s][1]), a1);
    }
    const float inv = 1.0f / L;
    reinterpret_cast<u32*>(lds + L_O)[v * 64 + lane] = pack2(a0 * inv, a1 * inv);
  }
  wg_barrier();
  if (ROLE <= 1) {
    const int ct = v;
    f32x4 acc4 = {0.f, 0.f, 0.f, 0.f};
#pragma unroll
    for (int s = 0; s < 4; ++s) {
      const u32x4 af = *reinterpret_cast<const u32x4*>(lds + L_O + c * 256 + (32 * s + 8 * r) * 2);
      acc4 = __builtin_amdgcn_mfma_f32_16x16x32_bf16(__builtin_bit_cast(s16x8, af), wof[s], acc4, 0, 0, 0);
    }
#pragma unroll
    for (int k = 0; k < 4; ++k) {
      const float nb = dpp<XOR1>(acc4[k]);
      if (lane < 32 && (c & 1) == 0)
        put_granule(rs, G_S + ((cj * NB + (4 * r + k)) * 32 + hi) * 32 + 8 * ct + (c >> 1), __float_as_uint(acc4[k]), __float_as_uint(nb), tag);
    }
  }
  if (ROLE == 2 && hi < 8 && v == 4) {
    const u32 sg = G_S + (cj * NB + hi) * 32 * 32;
    float s0 = 0.f, s1 = 0.f;
    for (u32 spins = 0; spins < SPIN_CAP; ++spins) {
      asm volatile("" ::: "memory");
      bool ok = true; s0 = 0.f; s1 = 0.f;
#pragma unroll
      for (int i = 0; i < 16; ++i) { const u32x4 g = get_granule(rs, sg + lane + 64 * i); s0 += __uint_as_float(g[0]); s1 += __uint_as_float(g[1]); ok &= tag_ok(g, tag); }
      if (__all(ok)) break;
      __builtin_amdgcn_s_sleep(2);
    }
    {
      auto t0 = __builtin_amdgcn_permlane32_swap(__float_as_uint(s0), __float_as_uint(s0), false, false);
      s0 = __uint_as_float(t0[0]) + __uint_as_float(t0[1]);
      auto t1 = __builtin_amdgcn_permlane32_swap(__float_as_uint(s1), __float_as_uint(s1), false, false);
      s1 = __uint_as_float(t1[0]) + __uint_as_float(t1[1]);
    }
    if (lane < 32) {
      const float g0 = reinterpret_cast<const float*>(lds + L_G)[2 * lane], g1 = reinterpret_cast<const float*>(lds + L_G)[2 * lane + 1];
      const float r0 = bflo(xp) + bf16r(bf16r(s0) * g0), r1 = bfhi(xp) + bf16r(bf16r(s1) * g1);
      *reinterpret_cast<u32*>(a.out + hi * DM + 64 * cj + 2 * lane) = pack2(r0, r1);
    }
    if (w == 0 && lane == 0)
      __hip_atomic_store(a.ws, ((u64)tag.hi << 32) | tag.lo, __ATOMIC_RELAXED, __HIP_MEMORY_SCOPE_AGENT);
  }
}

__global__ void __launch_bounds__(NTHR, 2) fused_decode_kernel(Params a) {
  __shared__ __attribute__((aligned(16))) unsigned char lds[L_END];
  const int v = __builtin_amdgcn_readfirstlane(threadIdx.x >> 6);
  if (v == 0) body<0>(a, lds, 0);
  else if (v < 4) body<1>(a, lds, v);
  else body<2>(a, lds, v);
}

extern "C" void kernel_launch(void* const* d_in, const int* in_sizes, int n_in,
                              void* d_out, int out_size, void* d_ws, size_t ws_size,
                              hipStream_t stream) {
  Params p;
  p.x = (const u16*)d_in[0];
  p.kc = (const u16*)d_in[1];
  p.vc = (const u16*)d_in[2];
  p.wq = (const u16*)d_in[3];
  p.wo = (const u16*)d_in[4];
  p.w1 = (const u16*)d_in[6];
  p.b1 = (const u16*)d_in[7];
  p.w2 = (const u16*)d_in[8];
  p.b2 = (const u16*)d_in[9];
  p.out = (u16*)d_out;
  p.ws = (u64*)d_ws;
  hipLaunchKernelGGL(fused_decode_kernel, dim3(NWG), dim3(NTHR), 0, stream, p);
}
```

```cpp
#include <hip/hip_runtime.h>
#include <hip/hip_bf16.h>
#include <cstdint>

typedef unsigned int u32;
typedef unsigned long long u64;
typedef unsigned short u16;
using u32x4 = __attribute__((ext_vector_type(4))) u32;
using f32x4 = __attribute__((ext_vector_type(4))) float;
using f32x2 = __attribute__((ext_vector_type(2))) float;
using s16x8 = __attribute__((ext_vector_type(8))) short;
using bf16x2 = __attribute__((ext_vector_type(2))) __bf16;

constexpr int NB = 8, DM = 512, HQ = 32, HKV = 8, SK = 4096, DH = 128, FF = 1792;
constexpr int NWG = 256, NTHR = 512;
constexpr u32 SPIN_CAP = 1u << 20;
constexpr float LOG2E = 1.4426950408889634f;

constexpr u32 G_Q = 16;
constexpr u32 G_H = G_Q + NB * 1024;
constexpr u32 G_P = G_H + NB * 448;
constexpr u32 G_S = G_P + NB * HQ * 4 * 66;
constexpr u32 G_Y = G_S + 8 * NB * 32 * 32;
constexpr u32 G_END = G_Y + 8 * NB * 32 * 32;

__device__ __forceinline__ float bf16r(float x) { return (float)(__bf16)x; }
__device__ __forceinline__ u32 pack2(float lo, float hi) {
  bf16x2 v = {(__bf16)lo, (__bf16)hi};
  return __builtin_bit_cast(u32, v);
}
__device__ __forceinline__ float bflo(u32 x) { return __builtin_bit_cast(float, x << 16); }
__device__ __forceinline__ float bfhi(u32 x) { return __builtin_bit_cast(float, x & 0xffff0000u); }
__device__ __forceinline__ float bfu(u16 x) { return __builtin_bit_cast(float, (u32)x << 16); }
__device__ __forceinline__ float dot2(u32 k, u32 q, float acc) {
  return __builtin_amdgcn_fdot2_f32_bf16(__builtin_bit_cast(bf16x2, k), __builtin_bit_cast(bf16x2, q), acc, false);
}
template <int CTRL> __device__ __forceinline__ float dpp(float x) {
  return __builtin_bit_cast(float, __builtin_amdgcn_mov_dpp(__builtin_bit_cast(int, x), CTRL, 0xf, 0xf, true));
}
constexpr int XOR1 = 0xB1, XOR2 = 0x4E, XOR7 = 0x141, XOR8 = 0x128, ROR4 = 0x124;
__device__ __forceinline__ float xrow16_max(float x) {
  auto s = __builtin_amdgcn_permlane16_swap(__float_as_uint(x), __float_as_uint(x), false, false);
  x = fmaxf(__uint_as_float(s[0]), __uint_as_float(s[1]));
  auto t = __builtin_amdgcn_permlane32_swap(__float_as_uint(x), __float_as_uint(x), false, false);
  return fmaxf(__uint_as_float(t[0]), __uint_as_float(t[1]));
}
__device__ __forceinline__ float xrow16_sum(float x) {
  auto s = __builtin_amdgcn_permlane16_swap(__float_as_uint(x), __float_as_uint(x), false, false);
  x = __uint_as_float(s[0]) + __uint_as_float(s[1]);
  auto t = __builtin_amdgcn_permlane32_swap(__float_as_uint(x), __float_as_uint(x), false, false);
  return __uint_as_float(t[0]) + __uint_as_float(t[1]);
}
typedef __amdgpu_buffer_rsrc_t rsrc_t;
struct Tag { u32 lo, hi; };
__device__ __forceinline__ void put_granule(rsrc_t rs, u32 g, u32 v0, u32 v1, Tag t) {
  __builtin_amdgcn_raw_buffer_store_b128(u32x4{v0, v1, t.lo, t.hi}, rs, (int)(g * 16u), 0, 16);
}
__device__ __forceinline__ u32x4 get_granule(rsrc_t rs, u32 g) {
  return __builtin_amdgcn_raw_buffer_load_b128(rs, (int)(g * 16u), 0, 16);
}
__device__ __forceinline__ bool tag_ok(u32x4 g, Tag t) { return (g[2] == t.lo) & (g[3] == t.hi); }
__device__ __forceinline__ void wg_barrier() {
  asm volatile("s_waitcnt lgkmcnt(0)" ::: "memory");
  __builtin_amdgcn_s_barrier();
  asm volatile("" ::: "memory");
}
__device__ __forceinline__ u32x4 ld_nt(const u16* p) {
  return __builtin_nontemporal_load(reinterpret_cast<const u32x4*>(p));
}
__device__ __forceinline__ u32 lds_flag_load(u32* f) {
  return (u32)__builtin_amdgcn_readfirstlane(__hip_atomic_load(f, __ATOMIC_RELAXED, __HIP_MEMORY_SCOPE_WORKGROUP));
}
__device__ __forceinline__ void lds_wait_ge(u32* f, u32 want) {
  for (u32 spins = 0; spins < SPIN_CAP * 8u; ++spins) {
    if (lds_flag_load(f) >= want) break;
    __builtin_amdgcn_s_sleep(1);
  }
  asm volatile("" ::: "memory");
}
__device__ __forceinline__ void probe_wait(rsrc_t rs, u32 g, bool active, Tag t) {
  for (u32 spins = 0; spins < SPIN_CAP; ++spins) {
    asm volatile("" ::: "memory");
    bool ok = true;
    if (active) ok = tag_ok(get_granule(rs, g), t);
    if (__all(ok)) break;
    __builtin_amdgcn_s_sleep(2);
  }
}

struct Params {
  const u16* x; const u16* kc; const u16* vc; const u16* wq; const u16* wo;
  const u16* w1; const u16* b1; const u16* w2; const u16* b2;
  u16* out; u64* ws;
};

constexpr int WROW = 144;
constexpr int L_WO = 0;
constexpr int L_W2 = L_WO + 128 * WROW;
constexpr int L_SVC = L_W2 + 64 * WROW;
constexpr int L_Q = L_SVC + 4 * 2 * 8 * 16 * 4;
constexpr int L_H = L_Q + 1024;
constexpr int L_O = L_H + 16 * 32 * 4;
constexpr int L_G = L_O + 16 * 64 * 4;
constexpr int L_FLAG = L_G + 256;
constexpr int L_P = L_FLAG + 32;
constexpr int L_MRG = L_P + 8 * 144 * 4 * 4;
constexpr int L_WQS = L_MRG + 8 * 4 * 132 * 4;
constexpr int L_W1S = L_WQS + 4 * 4352;
constexpr int L_XN = L_W1S + 4 * 2304;
constexpr int L_KT = L_XN + 64;
constexpr int L_END = L_KT + 8 * 4352;

template <int ROLE>
__device__ __forceinline__ void body(const Params& a, unsigned char* lds, const int v) {
  constexpr int NG = (ROLE == 0) ? 1 : 9;
  const int w = blockIdx.x;
  const int tid = threadIdx.x, lane = tid & 63;
  const int r = lane >> 4, c = lane & 15;
  const int bh = w >> 2, split = w & 3;
  const int ab = bh >> 3, ahkv = bh & 7;
  const int hi = w >> 3, cj = w & 7;
  const int qb = (w & 7) * 32 + (w >> 3);
  const bool h_on = (qb < 224);
  const rsrc_t rs = __builtin_amdgcn_make_buffer_rsrc((void*)a.ws, 0, (int)(G_END * 16u), 0x00020000);
  Tag tag = {0u, 0u};
  u64 epoch_raw = 0;
  if (ROLE == 0) epoch_raw = __hip_atomic_load(a.ws, __ATOMIC_RELAXED, __HIP_MEMORY_SCOPE_AGENT);
  u32* flags = reinterpret_cast<u32*>(lds + L_FLAG);

  if (ROLE == 0) {
    if (lane < 6) flags[lane] = 0u;
  }
  wg_barrier();
  __builtin_amdgcn_s_setprio(3);

  const int key0 = (ROLE == 0) ? 0 : 16 + 144 * (v - 1);
  const int kvoff = (r * DH + 8 * c) * 2;
  const int kvs = __builtin_amdgcn_readfirstlane((bh * SK + split * 1024 + key0) * DH * 2);
  const rsrc_t rk = __builtin_amdgcn_make_buffer_rsrc((void*)a.kc, 0, NB * HKV * SK * DH * 2, 0x00020000);
  const rsrc_t rv = __builtin_amdgcn_make_buffer_rsrc((void*)a.vc, 0, NB * HKV * SK * DH * 2, 0x00020000);
  constexpr int VPG = (ROLE == 0) ? 0 : 3;
  u32x4 kv[4 * NG];
  u32x4 vpre[4 * VPG + 1];
  u32 b1v = 0;

  if (ROLE <= 1) {
    u32x4 xs[2], xa[4];
#pragma unroll
    for (int i = 0; i < 2; ++i) xs[i] = ld_nt(a.x + (2 * v + i) * DM + 8 * lane);
#pragma unroll
    for (int s = 0; s < 4; ++s) xa[s] = ld_nt(a.x + (c & 7) * DM + 128 * v + 32 * s + 8 * r);
    u32x4 wqr[4], w1r[2];
#pragma unroll
    for (int u = 0; u < 4; ++u)
      wqr[u] = ld_nt(a.wq + (size_t)(128 * v + 32 * u + (lane >> 1)) * 4096 + 16 * qb + 8 * (lane & 1));
#pragma unroll
    for (int u = 0; u < 2; ++u)
      w1r[u] = ld_nt(a.w1 + (size_t)(128 * v + 64 * u + lane) * FF + 8 * (h_on ? qb : 0));
    if (ROLE == 0) b1v = a.b1[8 * (h_on ? qb : 0) + (lane & 7)];
    __builtin_amdgcn_sched_barrier(0);
    if (lane == 0) __hip_atomic_fetch_add(flags + 3, 1u, __ATOMIC_RELAXED, __HIP_MEMORY_SCOPE_WORKGROUP);
    float* stat = reinterpret_cast<float*>(lds + L_XN);
#pragma unroll
    for (int i = 0; i < 2; ++i) {
      float s = 0.f;
#pragma unroll
      for (int k = 0; k < 4; ++k) s += bflo(xs[i][k]) + bfhi(xs[i][k]);
      s += dpp<XOR1>(s); s += dpp<XOR2>(s); s += dpp<XOR7>(s); s += dpp<XOR8>(s); s = xrow16_sum(s);
      const float mean = s * (1.0f / DM);
      float q2 = 0.f;
#pragma unroll
      for (int k = 0; k < 4; ++k) {
        const float d0 = bflo(xs[i][k]) - mean, d1 = bfhi(xs[i][k]) - mean;
        q2 = fmaf(d0, d0, q2); q2 = fmaf(d1, d1, q2);
      }
      q2 += dpp<XOR1>(q2); q2 += dpp<XOR2>(q2); q2 += dpp<XOR7>(q2); q2 += dpp<XOR8>(q2); q2 = xrow16_sum(q2);
      const float var_bf = bf16r(q2 * (1.0f / DM));
      if (lane == 0) { stat[4 * v + 2 * i] = bf16r(mean); stat[4 * v + 2 * i + 1] = bf16r(sqrtf(bf16r(var_bf + 1e-5f))); }
    }
    asm volatile("s_waitcnt lgkmcnt(0)" ::: "memory");
    if (lane == 0) __hip_atomic_fetch_add(flags + 4, 1u, __ATOMIC_RELAXED, __HIP_MEMORY_SCOPE_WORKGROUP);
    unsigned char* wqs = lds + L_WQS + v * 4352;
    unsigned char* w1s = lds + L_W1S + v * 2304;
#pragma unroll
    for (int u = 0; u < 4; ++u) {
      const int rr = 32 * u + (lane >> 1);
      *reinterpret_cast<u32x4*>(wqs + rr * 32 + (rr >> 3) * 16 + 16 * (lane & 1)) = wqr[u];
    }
#pragma unroll
    for (int u = 0; u < 2; ++u) {
      const int rr = 64 * u + lane;
      *reinterpret_cast<u32x4*>(w1s + rr * 16 + (rr >> 3) * 16) = w1r[u];
    }
    lds_wait_ge(flags + 4, 4u);
    const float mean_bf = stat[2 * (c & 7)], sd_bf = stat[2 * (c & 7) + 1];
    asm volatile("s_waitcnt lgkmcnt(0)" ::: "memory");
    f32x4 accq = {0.f, 0.f, 0.f, 0.f}, acch = {0.f, 0.f, 0.f, 0.f};
#pragma unroll
    for (int s = 0; s < 4; ++s) {
      u32x4 af;
#pragma unroll
      for (int k = 0; k < 4; ++k)
        af[k] = (c < 8) ? pack2(bf16r(bflo(xa[s][k]) - mean_bf) / sd_bf, bf16r(bfhi(xa[s][k]) - mean_bf) / sd_bf) : 0u;
      const s16x8 afr = __builtin_bit_cast(s16x8, af);
      s16x8 bq, bws;
#pragma unroll
      for (int jj = 0; jj < 8; ++jj) {
        const int k = 32 * s + 8 * r + jj;
        bq[jj] = *reinterpret_cast<const short*>(wqs + k * 32 + (k >> 3) * 16 + c * 2);
        bws[jj] = *reinterpret_cast<const short*>(w1s + k * 16 + (k >> 3) * 16 + (c & 7) * 2);
      }
      if (c >= 8) bws = s16x8{0, 0, 0, 0, 0, 0, 0, 0};
      accq = __builtin_amdgcn_mfma_f32_16x16x32_bf16(afr, bq, accq, 0, 0, 0);
      acch = __builtin_amdgcn_mfma_f32_16x16x32_bf16(afr, bws, acch, 0, 0, 0);
    }
    float* svc = reinterpret_cast<float*>(lds + L_SVC) + v * 256;
    if (lane < 32) {
#pragma unroll
      for (int k = 0; k < 4; ++k) {
        svc[(4 * r + k) * 16 + c] = accq[k];
        svc[128 + (4 * r + k) * 16 + c] = acch[k];
      }
    }
    if (ROLE == 1) {
      asm volatile("s_waitcnt lgkmcnt(0)" ::: "memory");
      if (lane == 0) __hip_atomic_fetch_add(flags + 1, 1u, __ATOMIC_RELAXED, __HIP_MEMORY_SCOPE_WORKGROUP);
    }
  }

  if (ROLE == 2) {
    const int t4 = tid - 256;
    lds_wait_ge(flags + 3, 4u);
    u32x4 st[6];
#pragma unroll
    for (int u = 0; u < 4; ++u) {
      const int e = t4 + 256 * u, row = e >> 3, seg = e & 7;
      st[u] = ld_nt(a.wo + (size_t)(128 * hi + row) * DM + 64 * cj + 8 * seg);
    }
#pragma unroll
    for (int u = 0; u < 2; ++u) {
      const int e = t4 + 256 * u, row = e >> 3, seg = e & 7;
      st[4 + u] = ld_nt(a.w2 + (size_t)(56 * hi + (row < 56 ? row : 0)) * DM + 64 * cj + 8 * seg);
    }
#pragma unroll
    for (int i = 0; i < 4 * NG; ++i) kv[i] = __builtin_amdgcn_raw_buffer_load_b128(rk, kvoff, kvs + i * 1024, 2);
#pragma unroll
    for (int i = 0; i < 4 * VPG; ++i) vpre[i] = __builtin_amdgcn_raw_buffer_load_b128(rv, kvoff, kvs + i * 1024, 2);
    __builtin_amdgcn_sched_barrier(0);
#pragma unroll
    for (int u = 0; u < 4; ++u) {
      const int e = t4 + 256 * u, row = e >> 3, seg = e & 7;
      *reinterpret_cast<u32x4*>(lds + L_WO + row * WROW + 16 * seg) = st[u];
    }
#pragma unroll
    for (int u = 0; u < 2; ++u) {
      const int e = t4 + 256 * u, row = e >> 3, seg = e & 7;
      *reinterpret_cast<u32x4*>(lds + L_W2 + row * WROW + 16 * seg) = row < 56 ? st[4 + u] : u32x4{0u, 0u, 0u, 0u};
    }
    reinterpret_cast<u32*>(lds + L_H)[256 + t4] = 0u;
    reinterpret_cast<u32*>(lds + L_O)[512 + t4] = 0u;
    reinterpret_cast<u32*>(lds + L_O)[768 + t4] = 0u;
    asm volatile("s_waitcnt lgkmcnt(0)" ::: "memory");
    if (lane == 0) __hip_atomic_fetch_add(flags + 2, 1u, __ATOMIC_RELAXED, __HIP_MEMORY_SCOPE_WORKGROUP);
  }
  if (ROLE == 1) {
#pragma unroll
    for (int i = 0; i < 4 * NG; ++i) kv[i] = __builtin_amdgcn_raw_buffer_load_b128(rk, kvoff, kvs + i * 1024, 2);
#pragma unroll
    for (int i = 0; i < 4 * VPG; ++i) vpre[i] = __builtin_amdgcn_raw_buffer_load_b128(rv, kvoff, kvs + i * 1024, 2);
    __builtin_amdgcn_sched_barrier(0);
  }

  if (ROLE == 0) {
    lds_wait_ge(flags + 1, 3u);
    {
      const u64 e = epoch_raw + 1ull;
      tag.lo = (u32)__builtin_amdgcn_readfirstlane((u32)e);
      tag.hi = (u32)__builtin_amdgcn_readfirstlane((u32)(e >> 32));
      if (lane == 0) { flags[6] = tag.lo; flags[7] = tag.hi; }
    }
    const float* svc = reinterpret_cast<const float*>(lds + L_SVC);
    if (lane < 32) {
      const int b = lane >> 2, cq = lane & 3;
      float sv[4];
#pragma unroll
      for (int e = 0; e < 4; ++e) {
        float s0 = 0.f;
#pragma unroll
        for (int u = 0; u < 4; ++u) s0 += svc[u * 256 + b * 16 + 4 * cq + e];
        sv[e] = s0;
      }
      put_granule(rs, G_Q + b * 1024 + 4 * qb + cq, pack2(sv[0], sv[1]), pack2(sv[2], sv[3]), tag);
    }
    {
      const int b = lane >> 3, col = lane & 7;
      float s0 = 0.f;
#pragma unroll
      for (int u = 0; u < 4; ++u) s0 += svc[u * 256 + 128 + b * 16 + col];
      const float t = bf16r(bf16r(s0) + bfu((u16)b1v));
      const float inner = 0.7978845608028654f * (t + 0.044715f * t * t * t);
      const float hv = 0.5f * t * (1.0f + tanhf(inner));
      const float h1 = dpp<XOR1>(hv);
      const u32 pr = (lane & 1) ? pack2(h1, hv) : pack2(hv, h1);
      const u32 pr2 = __builtin_bit_cast(u32, dpp<XOR2>(__builtin_bit_cast(float, pr)));
      if (h_on && (lane & 3) == 0) put_granule(rs, G_H + b * 448 + 2 * qb + (col >> 2), pr, pr2, tag);
    }
    u32x4 gq[2], gh[2];
    for (u32 spins = 0; spins < SPIN_CAP; ++spins) {
      asm volatile("" ::: "memory");
      bool ok = true;
#pragma unroll
      for (int k = 0; k < 2; ++k) { gq[k] = get_granule(rs, G_Q + ab * 1024 + ahkv * 128 + 64 * k + lane); ok &= tag_ok(gq[k], tag); }
      if (__all(ok)) break;
      __builtin_amdgcn_s_sleep(1);
    }
    u64* qs = reinterpret_cast<u64*>(lds + L_Q);
#pragma unroll
    for (int k = 0; k < 2; ++k) qs[64 * k + lane] = ((u64)gq[k][1] << 32) | gq[k][0];
    asm volatile("s_waitcnt lgkmcnt(0)" ::: "memory");
    if (lane == 0) __hip_atomic_store(flags, 1u, __ATOMIC_RELAXED, __HIP_MEMORY_SCOPE_WORKGROUP);
#pragma unroll
    for (int i = 0; i < 4 * NG; ++i) kv[i] = __builtin_amdgcn_raw_buffer_load_b128(rk, kvoff, kvs + i * 1024, 2);
    u32 hg[2]; int hidx[2];
#pragma unroll
    for (int k = 0; k < 2; ++k) {
      const int idx = min(lane + 64 * k, 111);
      const int b = idx / 14, t = idx % 14;
      hg[k] = G_H + b * 448 + 14 * hi + t;
      hidx[k] = b * 16 + t;
    }
    for (u32 spins = 0; spins < SPIN_CAP; ++spins) {
      asm volatile("" ::: "memory");
      bool ok = true;
#pragma unroll
      for (int k = 0; k < 2; ++k) { gh[k] = get_granule(rs, hg[k]); ok &= tag_ok(gh[k], tag); }
      if (__all(ok)) break;
      __builtin_amdgcn_s_sleep(1);
    }
    u64* hs = reinterpret_cast<u64*>(lds + L_H);
#pragma unroll
    for (int k = 0; k < 2; ++k) if (lane + 64 * k < 112) hs[hidx[k]] = ((u64)gh[k][1] << 32) | gh[k][0];
    if (lane < 16) hs[(lane >> 1) * 16 + 14 + (lane & 1)] = 0ull;
    lds_wait_ge(flags + 2, 4u);
    asm volatile("s_waitcnt lgkmcnt(0)" ::: "memory");
#pragma unroll
    for (int ct = 0; ct < 4; ++ct) {
      f32x4 acc = {0.f, 0.f, 0.f, 0.f};
#pragma unroll
      for (int s = 0; s < 2; ++s) {
        const u32x4 af = *reinterpret_cast<const u32x4*>(lds + L_H + c * 128 + (32 * s + 8 * r) * 2);
        s16x8 bf;
#pragma unroll
        for (int jj = 0; jj < 8; ++jj)
          bf[jj] = *reinterpret_cast<const short*>(lds + L_W2 + (32 * s + 8 * r + jj) * WROW + (16 * ct + c) * 2);
        acc = __builtin_amdgcn_mfma_f32_16x16x32_bf16(__builtin_bit_cast(s16x8, af), bf, acc, 0, 0, 0);
      }
#pragma unroll
      for (int k = 0; k < 4; ++k) {
        const float nb = dpp<XOR1>(acc[k]);
        if (lane < 32 && (c & 1) == 0)
          put_granule(rs, G_Y + ((cj * NB + (4 * r + k)) * 32 + hi) * 32 + 8 * ct + (c >> 1), __float_as_uint(acc[k]), __float_as_uint(nb), tag);
      }
    }
  } else {
    __builtin_amdgcn_s_setprio(0);
    lds_wait_ge(flags, 1u);
  }

  s16x8 qf[4];
#pragma unroll
  for (int s = 0; s < 4; ++s) {
    u32x4 t = *reinterpret_cast<const u32x4*>(lds + L_Q + (c & 3) * 256 + 64 * s + 16 * r);
    if (c >= 4) t = u32x4{0u, 0u, 0u, 0u};
    qf[s] = __builtin_bit_cast(s16x8, t);
  }
  float sc[NG][4];
  unsigned char* ktile = lds + L_KT + v * 4352;
#pragma unroll
  for (int grp = 0; grp < NG; ++grp) {
#pragma unroll
    for (int t = 0; t < 4; ++t) *reinterpret_cast<u32x4*>(ktile + (4 * t + r) * 272 + c * 16) = kv[4 * grp + t];
    asm volatile("s_waitcnt lgkmcnt(0)" ::: "memory");
    u32x4 kf[4];
#pragma unroll
    for (int s = 0; s < 4; ++s) kf[s] = *reinterpret_cast<const u32x4*>(ktile + c * 272 + (4 * s + r) * 16);
    asm volatile("s_waitcnt lgkmcnt(0)" ::: "memory");
    f32x4 d = {0.f, 0.f, 0.f, 0.f};
#pragma unroll
    for (int s = 0; s < 4; ++s) d = __builtin_amdgcn_mfma_f32_16x16x32_bf16(__builtin_bit_cast(s16x8, kf[s]), qf[s], d, 0, 0, 0);
#pragma unroll
    for (int k = 0; k < 4; ++k) sc[grp][k] = bf16r(bf16r(d[k]) * 0.08838834764831845f);
  }
  float m = sc[0][0];
#pragma unroll
  for (int grp = 0; grp < NG; ++grp)
#pragma unroll
    for (int k = 0; k < 4; ++k) m = fmaxf(m, sc[grp][k]);
  m = xrow16_max(m);
  float l = 0.f;
  {
    float* pl = reinterpret_cast<float*>(lds + L_P) + v * 576;
#pragma unroll
    for (int grp = 0; grp < NG; ++grp)
#pragma unroll
      for (int k = 0; k < 4; ++k) {
        const float p = __builtin_amdgcn_exp2f((sc[grp][k] - m) * LOG2E);
        l += p;
        if (c < 4) pl[(16 * grp + 4 * r + k) * 4 + c] = p;
      }
  }
  l = xrow16_sum(l);
  asm volatile("s_waitcnt lgkmcnt(0)" ::: "memory");

  constexpr int VAH = 3;
  f32x2 acc2[16];
#pragma unroll
  for (int k = 0; k < 16; ++k) acc2[k] = f32x2{0.f, 0.f};
  {
    const f32x4* pl4 = reinterpret_cast<const f32x4*>(lds + L_P) + v * 144;
#pragma unroll
    for (int grp = 0; grp < NG; ++grp) {
#pragma unroll
      for (int gg = (grp == 0 ? VPG : VPG + VAH - 1 + grp); gg <= VPG + VAH - 1 + grp; ++gg) {
        if (gg < NG) {
#pragma unroll
          for (int t = 0; t < 4; ++t) kv[4 * (gg - VPG) + t] = __builtin_amdgcn_raw_buffer_load_b128(rv, kvoff, kvs + (4 * gg + t) * 1024, 2);
        }
      }
      __builtin_amdgcn_sched_barrier(0);
#pragma unroll
      for (int t = 0; t < 4; ++t) {
        const int i = 4 * grp + t;
        const f32x4 p = pl4[4 * i + r];
#pragma unroll
        for (int k = 0; k < 4; ++k) {
          const u32 vv = (i < 4 * VPG) ? vpre[i < 4 * VPG ? i : 0][k] : kv[i >= 4 * VPG ? i - 4 * VPG : 0][k];
          const f32x2 vf = {bflo(vv), bfhi(vv)};
#pragma unroll
          for (int g = 0; g < 4; ++g) acc2[g * 4 + k] = __builtin_elementwise_fma(f32x2{p[g], p[g]}, vf, acc2[g * 4 + k]);
        }
      }
      __builtin_amdgcn_sched_barrier(0);
    }
  }
  float acc[32];
#pragma unroll
  for (int k = 0; k < 32; ++k) acc[k] = xrow16_sum(acc2[k >> 1][k & 1]);
  {
    float* mrg = reinterpret_cast<float*>(lds + L_MRG) + v * 4 * 132;
    if (r == 0) {
#pragma unroll
      for (int g = 0; g < 4; ++g) {
        *reinterpret_cast<f32x4*>(mrg + g * 132 + 4 + 8 * c) = f32x4{acc[g * 8], acc[g * 8 + 1], acc[g * 8 + 2], acc[g * 8 + 3]};
        *reinterpret_cast<f32x4*>(mrg + g * 132 + 4 + 8 * c + 4) = f32x4{acc[g * 8 + 4], acc[g * 8 + 5], acc[g * 8 + 6], acc[g * 8 + 7]};
      }
      if (c < 4) { mrg[c * 132] = m; mrg[c * 132 + 1] = l; }
    }
  }
  if (ROLE == 0 && hi < 8) {
    const u32 yg = G_Y + (cj * NB + hi) * 32 * 32 + (lane & 31) + (lane >> 5) * 32;
    float y0 = 0.f, y1 = 0.f;
    for (u32 spins = 0; spins < SPIN_CAP; ++spins) {
      asm volatile("" ::: "memory");
      bool ok = true; y0 = 0.f; y1 = 0.f;
#pragma unroll
      for (int i = 0; i < 16; ++i) { const u32x4 g = get_granule(rs, yg + 64 * i); y0 += __uint_as_float(g[0]); y1 += __uint_as_float(g[1]); ok &= tag_ok(g, tag); }
      if (__all(ok)) break;
      __builtin_amdgcn_s_sleep(8);
    }
    {
      auto t0 = __builtin_amdgcn_permlane32_swap(__float_as_uint(y0), __float_as_uint(y0), false, false);
      y0 = __uint_as_float(t0[0]) + __uint_as_float(t0[1]);
      auto t1 = __builtin_amdgcn_permlane32_swap(__float_as_uint(y1), __float_as_uint(y1), false, false);
      y1 = __uint_as_float(t1[0]) + __uint_as_float(t1[1]);
    }
    if (lane < 32) {
      const u32 b2p = *reinterpret_cast<const u32*>(a.b2 + 64 * cj + 2 * lane);
      const float ya = bf16r(bf16r(y0) + bflo(b2p)), yb = bf16r(bf16r(y1) + bfhi(b2p));
      reinterpret_cast<float*>(lds + L_G)[2 * lane] = bf16r(ya / (1.0f + __expf(-ya)));
      reinterpret_cast<float*>(lds + L_G)[2 * lane + 1] = bf16r(yb / (1.0f + __expf(-yb)));
    }
  }
  wg_barrier();
  if (ROLE != 0) {
    tag.lo = (u32)__builtin_amdgcn_readfirstlane(flags[6]);
    tag.hi = (u32)__builtin_amdgcn_readfirstlane(flags[7]);
  }

  if (tid < 256) {
    const int g = tid >> 6, d2 = tid & 63;
    const float* mrg = reinterpret_cast<const float*>(lds + L_MRG) + g * 132;
    float M = mrg[0];
#pragma unroll
    for (int u = 1; u < 8; ++u) M = fmaxf(M, mrg[u * 4 * 132]);
    float L = 0.f, o0 = 0.f, o1 = 0.f;
#pragma unroll
    for (int u = 0; u < 8; ++u) {
      const float wt = __builtin_amdgcn_exp2f((mrg[u * 4 * 132] - M) * LOG2E);
      L = fmaf(wt, mrg[u * 4 * 132 + 1], L);
      o0 = fmaf(wt, mrg[u * 4 * 132 + 4 + 2 * d2], o0);
      o1 = fmaf(wt, mrg[u * 4 * 132 + 5 + 2 * d2], o1);
    }
    const u32 pg = G_P + ((ab * HQ + ahkv * 4 + g) * 4 + split) * 66;
    put_granule(rs, pg + d2, __float_as_uint(o0), __float_as_uint(o1), tag);
    if (d2 == 0) put_granule(rs, pg + 64, __float_as_uint(M), __float_as_uint(L), tag);
  }

  s16x8 wof[4];
  if (ROLE <= 1) {
#pragma unroll
    for (int s = 0; s < 4; ++s)
#pragma unroll
      for (int jj = 0; jj < 8; ++jj)
        wof[s][jj] = *reinterpret_cast<const short*>(lds + L_WO + (32 * s + 8 * r + jj) * WROW + (16 * v + c) * 2);
  }
  u32 xp = 0;
  if (ROLE == 2 && hi < 8 && v == 4) xp = *reinterpret_cast<const u32*>(a.x + hi * DM + 64 * cj + 2 * (lane & 31));
  {
    const u32 pg = G_P + (v * HQ + hi) * 4 * 66;
    probe_wait(rs, pg + (lane & 3) * 66 + 64, lane < 4, tag);
    u32x4 go[4], gm[4];
    for (u32 spins = 0; spins < SPIN_CAP; ++spins) {
      asm volatile("" ::: "memory");
      bool ok = true;
#pragma unroll
      for (int s = 0; s < 4; ++s) {
        go[s] = get_granule(rs, pg + s * 66 + lane);
        gm[s] = get_granule(rs, pg + s * 66 + 64);
        ok &= tag_ok(go[s], tag) & tag_ok(gm[s], tag);
      }
      if (__all(ok)) break;
      __builtin_amdgcn_s_sleep(2);
    }
    float M = __uint_as_float(gm[0][0]);
#pragma unroll
    for (int s = 1; s < 4; ++s) M = fmaxf(M, __uint_as_float(gm[s][0]));
    float L = 0.f, a0 = 0.f, a1 = 0.f;
#pragma unroll
    for (int s = 0; s < 4; ++s) {
      const float wt = __builtin_amdgcn_exp2f((__uint_as_float(gm[s][0]) - M) * LOG2E);
      L = fmaf(wt, __uint_as_float(gm[s][1]), L);
      a0 = fmaf(wt, __uint_as_float(go[s][0]), a0);
      a1 = fmaf(wt, __uint_as_float(go[s][1]), a1);
    }
    const float inv = 1.0f / L;
    reinterpret_cast<u32*>(lds + L_O)[v * 64 + lane] = pack2(a0 * inv, a1 * inv);
  }
  wg_barrier();
  if (ROLE <= 1) {
    const int ct = v;
    f32x4 acc4 = {0.f, 0.f, 0.f, 0.f};
#pragma unroll
    for (int s = 0; s < 4; ++s) {
      const u32x4 af = *reinterpret_cast<const u32x4*>(lds + L_O + c * 256 + (32 * s + 8 * r) * 2);
      acc4 = __builtin_amdgcn_mfma_f32_16x16x32_bf16(__builtin_bit_cast(s16x8, af), wof[s], acc4, 0, 0, 0);
    }
#pragma unroll
    for (int k = 0; k < 4; ++k) {
      const float nb = dpp<XOR1>(acc4[k]);
      if (lane < 32 && (c & 1) == 0)
        put_granule(rs, G_S + ((cj * NB + (4 * r + k)) * 32 + hi) * 32 + 8 * ct + (c >> 1), __float_as_uint(acc4[k]), __float_as_uint(nb), tag);
    }
  }
  if (ROLE == 2 && hi < 8 && v == 4) {
    const u32 sg = G_S + (cj * NB + hi) * 32 * 32;
    float s0 = 0.f, s1 = 0.f;
    for (u32 spins = 0; spins < SPIN_CAP; ++spins) {
      asm volatile("" ::: "memory");
      bool ok = true; s0 = 0.f; s1 = 0.f;
#pragma unroll
      for (int i = 0; i < 16; ++i) { const u32x4 g = get_granule(rs, sg + lane + 64 * i); s0 += __uint_as_float(g[0]); s1 += __uint_as_float(g[1]); ok &= tag_ok(g, tag); }
      if (__all(ok)) break;
      __builtin_amdgcn_s_sleep(2);
    }
    {
      auto t0 = __builtin_amdgcn_permlane32_swap(__float_as_uint(s0), __float_as_uint(s0), false, false);
      s0 = __uint_as_float(t0[0]) + __uint_as_float(t0[1]);
      auto t1 = __builtin_amdgcn_permlane32_swap(__float_as_uint(s1), __float_as_uint(s1), false, false);
      s1 = __uint_as_float(t1[0]) + __uint_as_float(t1[1]);
    }
    if (lane < 32) {
      const float g0 = reinterpret_cast<const float*>(lds + L_G)[2 * lane], g1 = reinterpret_cast<const float*>(lds + L_G)[2 * lane + 1];
      const float r0 = bflo(xp) + bf16r(bf16r(s0) * g0), r1 = bfhi(xp) + bf16r(bf16r(s1) * g1);
      *reinterpret_cast<u32*>(a.out + hi * DM + 64 * cj + 2 * lane) = pack2(r0, r1);
    }
    if (w == 0 && lane == 0)
      __hip_atomic_store(a.ws, ((u64)tag.hi << 32) | tag.lo, __ATOMIC_RELAXED, __HIP_MEMORY_SCOPE_AGENT);
  }
}

__global__ void __launch_bounds__(NTHR, 2) fused_decode_kernel(Params a) {
  __shared__ __attribute__((aligned(16))) unsigned char lds[L_END];
  const int v = __builtin_amdgcn_readfirstlane(threadIdx.x >> 6);
  if (v == 0) body<0>(a, lds, 0);
  else if (v < 4) body<1>(a, lds, v);
  else body<2>(a, lds, v);
}

extern "C" void kernel_launch(void* const* d_in, const int* in_sizes, int n_in,
                              void* d_out, int out_size, void* d_ws, size_t ws_size,
                              hipStream_t stream) {
  Params p;
  p.x = (const u16*)d_in[0];
  p.kc = (const u16*)d_in[1];
  p.vc = (const u16*)d_in[2];
  p.wq = (const u16*)d_in[3];
  p.wo = (const u16*)d_in[4];
  p.w1 = (const u16*)d_in[6];
  p.b1 = (const u16*)d_in[7];
  p.w2 = (const u16*)d_in[8];
  p.b2 = (const u16*)d_in[9];
  p.out = (u16*)d_out;
  p.ws = (u64*)d_ws;
  hipLaunchKernelGGL(fused_decode_kernel, dim3(NWG), dim3(NTHR), 0, stream, p);
}
```

```cpp
#include <hip/hip_runtime.h>
#include <hip/hip_bf16.h>
#include <cstdint>

typedef unsigned int u32;
typedef unsigned long long u64;
typedef unsigned short u16;
using u32x4 = __attribute__((ext_vector_type(4))) u32;
using f32x4 = __attribute__((ext_vector_type(4))) float;
using f32x2 = __attribute__((ext_vector_type(2))) float;
using s16x8 = __attribute__((ext_vector_type(8))) short;
using bf16x2 = __attribute__((ext_vector_type(2))) __bf16;

constexpr int NB = 8, DM = 512, HQ = 32, HKV = 8, SK = 4096, DH = 128, FF = 1792;
constexpr int NWG = 256, NTHR = 512;
constexpr u32 SPIN_CAP = 1u << 20;
constexpr float LOG2E = 1.4426950408889634f;

constexpr u32 G_Q = 16;
constexpr u32 G_H = G_Q + NB * 1024;
constexpr u32 G_P = G_H + NB * 448;
constexpr u32 G_S = G_P + NB * HQ * 4 * 66;
constexpr u32 G_Y = G_S + 8 * NB * 32 * 32;
constexpr u32 G_END = G_Y + 8 * NB * 32 * 32;

__device__ __forceinline__ float bf16r(float x) { return (float)(__bf16)x; }
__device__ __forceinline__ u32 pack2(float lo, float hi) {
  bf16x2 v = {(__bf16)lo, (__bf16)hi};
  return __builtin_bit_cast(u32, v);
}
__device__ __forceinline__ float bflo(u32 x) { return __builtin_bit_cast(float, x << 16); }
__device__ __forceinline__ float bfhi(u32 x) { return __builtin_bit_cast(float, x & 0xffff0000u); }
__device__ __forceinline__ float bfu(u16 x) { return __builtin_bit_cast(float, (u32)x << 16); }
__device__ __forceinline__ float dot2(u32 k, u32 q, float acc) {
  return __builtin_amdgcn_fdot2_f32_bf16(__builtin_bit_cast(bf16x2, k), __builtin_bit_cast(bf16x2, q), acc, false);
}
template <int CTRL> __device__ __forceinline__ float dpp(float x) {
  return __builtin_bit_cast(float, __builtin_amdgcn_mov_dpp(__builtin_bit_cast(int, x), CTRL, 0xf, 0xf, true));
}
constexpr int XOR1 = 0xB1, XOR2 = 0x4E, XOR7 = 0x141, XOR8 = 0x128, ROR4 = 0x124;
__device__ __forceinline__ float xrow16_max(float x) {
  auto s = __builtin_amdgcn_permlane16_swap(__float_as_uint(x), __float_as_uint(x), false, false);
  x = fmaxf(__uint_as_float(s[0]), __uint_as_float(s[1]));
  auto t = __builtin_amdgcn_permlane32_swap(__float_as_uint(x), __float_as_uint(x), false, false);
  return fmaxf(__uint_as_float(t[0]), __uint_as_float(t[1]));
}
__device__ __forceinline__ float xrow16_sum(float x) {
  auto s = __builtin_amdgcn_permlane16_swap(__float_as_uint(x), __float_as_uint(x), false, false);
  x = __uint_as_float(s[0]) + __uint_as_float(s[1]);
  auto t = __builtin_amdgcn_permlane32_swap(__float_as_uint(x), __float_as_uint(x), false, false);
  return __uint_as_float(t[0]) + __uint_as_float(t[1]);
}
typedef __amdgpu_buffer_rsrc_t rsrc_t;
struct Tag { u32 lo, hi; };
__device__ __forceinline__ void put_granule(rsrc_t rs, u32 g, u32 v0, u32 v1, Tag t) {
  __builtin_amdgcn_raw_buffer_store_b128(u32x4{v0, v1, t.lo, t.hi}, rs, (int)(g * 16u), 0, 16);
}
__device__ __forceinline__ u32x4 get_granule(rsrc_t rs, u32 g) {
  return __builtin_amdgcn_raw_buffer_load_b128(rs, (int)(g * 16u), 0, 16);
}
__device__ __forceinline__ bool tag_ok(u32x4 g, Tag t) { return (g[2] == t.lo) & (g[3] == t.hi); }
__device__ __forceinline__ void wg_barrier() {
  asm volatile("s_waitcnt lgkmcnt(0)" ::: "memory");
  __builtin_amdgcn_s_barrier();
  asm volatile("" ::: "memory");
}
__device__ __forceinline__ u32x4 ld_nt(const u16* p) {
  return __builtin_nontemporal_load(reinterpret_cast<const u32x4*>(p));
}
__device__ __forceinline__ u32 lds_flag_load(u32* f) {
  return (u32)__builtin_amdgcn_readfirstlane(__hip_atomic_load(f, __ATOMIC_RELAXED, __HIP_MEMORY_SCOPE_WORKGROUP));
}
__device__ __forceinline__ void lds_wait_ge(u32* f, u32 want) {
  for (u32 spins = 0; spins < SPIN_CAP * 8u; ++spins) {
    if (lds_flag_load(f) >= want) break;
    __builtin_amdgcn_s_sleep(1);
  }
  asm volatile("" ::: "memory");
}
__device__ __forceinline__ void probe_wait(rsrc_t rs, u32 g, bool active, Tag t) {
  for (u32 spins = 0; spins < SPIN_CAP; ++spins) {
    asm volatile("" ::: "memory");
    bool ok = true;
    if (active) ok = tag_ok(get_granule(rs, g), t);
    if (__all(ok)) break;
    __builtin_amdgcn_s_sleep(2);
  }
}

struct Params {
  const u16* x; const u16* kc; const u16* vc; const u16* wq; const u16* wo;
  const u16* w1; const u16* b1; const u16* w2; const u16* b2;
  u16* out; u64* ws;
};

constexpr int WROW = 144;
constexpr int L_WO = 0;
constexpr int L_W2 = L_WO + 128 * WROW;
constexpr int L_SVC = L_W2 + 64 * WROW;
constexpr int L_Q = L_SVC + 4 * 2 * 8 * 16 * 4;
constexpr int L_H = L_Q + 1024;
constexpr int L_O = L_H + 16 * 32 * 4;
constexpr int L_G = L_O + 16 * 64 * 4;
constexpr int L_FLAG = L_G + 256;
constexpr int L_P = L_FLAG + 32;
constexpr int L_MRG = L_P + 8 * 144 * 4 * 4;
constexpr int L_WQS = L_MRG + 8 * 4 * 132 * 4;
constexpr int L_W1S = L_WQS + 4 * 4352;
constexpr int L_XN = L_W1S + 4 * 2304;
constexpr int L_KT = L_XN + 64;
constexpr int L_END = L_KT + 8 * 4352;

template <int ROLE>
__device__ __forceinline__ void body(const Params& a, unsigned char* lds, const int v) {
  constexpr int NG = (ROLE == 0) ? 1 : 9;
  const int w = blockIdx.x;
  const int tid = threadIdx.x, lane = tid & 63;
  const int r = lane >> 4, c = lane & 15;
  const int bh = w >> 2, split = w & 3;
  const int ab = bh >> 3, ahkv = bh & 7;
  const int hi = w >> 3, cj = w & 7;
  const int qb = (w & 7) * 32 + (w >> 3);
  const bool h_on = (qb < 224);
  const rsrc_t rs = __builtin_amdgcn_make_buffer_rsrc((void*)a.ws, 0, (int)(G_END * 16u), 0x00020000);
  Tag tag = {0u, 0u};
  u64 epoch_raw = 0;
  if (ROLE == 0) epoch_raw = __hip_atomic_load(a.ws, __ATOMIC_RELAXED, __HIP_MEMORY_SCOPE_AGENT);
  u32* flags = reinterpret_cast<u32*>(lds + L_FLAG);

  if (ROLE == 0) {
    if (lane < 6) flags[lane] = 0u;
  }
  wg_barrier();
  __builtin_amdgcn_s_setprio(3);

  const int key0 = (ROLE == 0) ? 0 : 16 + 144 * (v - 1);
  const size_t kvbase = ((size_t)bh * SK + split * 1024 + key0) * DH + (size_t)r * DH + 8 * c;
  constexpr int VPG = (ROLE == 0) ? 0 : 3;
  u32x4 kv[4 * NG];
  u32x4 vpre[4 * VPG + 1];
  u32 b1v = 0;

  if (ROLE <= 1) {
    u32x4 xs[2], xa[4];
#pragma unroll
    for (int i = 0; i < 2; ++i) xs[i] = ld_nt(a.x + (2 * v + i) * DM + 8 * lane);
#pragma unroll
    for (int s = 0; s < 4; ++s) xa[s] = ld_nt(a.x + (c & 7) * DM + 128 * v + 32 * s + 8 * r);
    u32x4 wqr[4], w1r[2];
#pragma unroll
    for (int u = 0; u < 4; ++u)
      wqr[u] = ld_nt(a.wq + (size_t)(128 * v + 32 * u + (lane >> 1)) * 4096 + 16 * qb + 8 * (lane & 1));
#pragma unroll
    for (int u = 0; u < 2; ++u)
      w1r[u] = ld_nt(a.w1 + (size_t)(128 * v + 64 * u + lane) * FF + 8 * (h_on ? qb : 0));
    if (ROLE == 0) b1v = a.b1[8 * (h_on ? qb : 0) + (lane & 7)];
    __builtin_amdgcn_sched_barrier(0);
    if (lane == 0) __hip_atomic_fetch_add(flags + 3, 1u, __ATOMIC_RELAXED, __HIP_MEMORY_SCOPE_WORKGROUP);
    float* stat = reinterpret_cast<float*>(lds + L_XN);
#pragma unroll
    for (int i = 0; i < 2; ++i) {
      float s = 0.f;
#pragma unroll
      for (int k = 0; k < 4; ++k) s += bflo(xs[i][k]) + bfhi(xs[i][k]);
      s += dpp<XOR1>(s); s += dpp<XOR2>(s); s += dpp<XOR7>(s); s += dpp<XOR8>(s); s = xrow16_sum(s);
      const float mean = s * (1.0f / DM);
      float q2 = 0.f;
#pragma unroll
      for (int k = 0; k < 4; ++k) {
        const float d0 = bflo(xs[i][k]) - mean, d1 = bfhi(xs[i][k]) - mean;
        q2 = fmaf(d0, d0, q2); q2 = fmaf(d1, d1, q2);
      }
      q2 += dpp<XOR1>(q2); q2 += dpp<XOR2>(q2); q2 += dpp<XOR7>(q2); q2 += dpp<XOR8>(q2); q2 = xrow16_sum(q2);
      const float var_bf = bf16r(q2 * (1.0f / DM));
      if (lane == 0) { stat[4 * v + 2 * i] = bf16r(mean); stat[4 * v + 2 * i + 1] = bf16r(sqrtf(bf16r(var_bf + 1e-5f))); }
    }
    asm volatile("s_waitcnt lgkmcnt(0)" ::: "memory");
    if (lane == 0) __hip_atomic_fetch_add(flags + 4, 1u, __ATOMIC_RELAXED, __HIP_MEMORY_SCOPE_WORKGROUP);
    unsigned char* wqs = lds + L_WQS + v * 4352;
    unsigned char* w1s = lds + L_W1S + v * 2304;
#pragma unroll
    for (int u = 0; u < 4; ++u) {
      const int rr = 32 * u + (lane >> 1);
      *reinterpret_cast<u32x4*>(wqs + rr * 32 + (rr >> 3) * 16 + 16 * (lane & 1)) = wqr[u];
    }
#pragma unroll
    for (int u = 0; u < 2; ++u) {
      const int rr = 64 * u + lane;
      *reinterpret_cast<u32x4*>(w1s + rr * 16 + (rr >> 3) * 16) = w1r[u];
    }
    lds_wait_ge(flags + 4, 4u);
    const float mean_bf = stat[2 * (c & 7)], sd_bf = stat[2 * (c & 7) + 1];
    asm volatile("s_waitcnt lgkmcnt(0)" ::: "memory");
    f32x4 accq = {0.f, 0.f, 0.f, 0.f}, acch = {0.f, 0.f, 0.f, 0.f};
#pragma unroll
    for (int s = 0; s < 4; ++s) {
      u32x4 af;
#pragma unroll
      for (int k = 0; k < 4; ++k)
        af[k] = (c < 8) ? pack2(bf16r(bflo(xa[s][k]) - mean_bf) / sd_bf, bf16r(bfhi(xa[s][k]) - mean_bf) / sd_bf) : 0u;
      const s16x8 afr = __builtin_bit_cast(s16x8, af);
      s16x8 bq, bws;
#pragma unroll
      for (int jj = 0; jj < 8; ++jj) {
        const int k = 32 * s + 8 * r + jj;
        bq[jj] = *reinterpret_cast<const short*>(wqs + k * 32 + (k >> 3) * 16 + c * 2);
        bws[jj] = *reinterpret_cast<const short*>(w1s + k * 16 + (k >> 3) * 16 + (c & 7) * 2);
      }
      if (c >= 8) bws = s16x8{0, 0, 0, 0, 0, 0, 0, 0};
      accq = __builtin_amdgcn_mfma_f32_16x16x32_bf16(afr, bq, accq, 0, 0, 0);
      acch = __builtin_amdgcn_mfma_f32_16x16x32_bf16(afr, bws, acch, 0, 0, 0);
    }
    float* svc = reinterpret_cast<float*>(lds + L_SVC) + v * 256;
    if (lane < 32) {
#pragma unroll
      for (int k = 0; k < 4; ++k) {
        svc[(4 * r + k) * 16 + c] = accq[k];
        svc[128 + (4 * r + k) * 16 + c] = acch[k];
      }
    }
    if (ROLE == 1) {
      asm volatile("s_waitcnt lgkmcnt(0)" ::: "memory");
      if (lane == 0) __hip_atomic_fetch_add(flags + 1, 1u, __ATOMIC_RELAXED, __HIP_MEMORY_SCOPE_WORKGROUP);
    }
  }

  if (ROLE == 2) {
    const int t4 = tid - 256;
    lds_wait_ge(flags + 3, 4u);
    u32x4 st[6];
#pragma unroll
    for (int u = 0; u < 4; ++u) {
      const int e = t4 + 256 * u, row = e >> 3, seg = e & 7;
      st[u] = ld_nt(a.wo + (size_t)(128 * hi + row) * DM + 64 * cj + 8 * seg);
    }
#pragma unroll
    for (int u = 0; u < 2; ++u) {
      const int e = t4 + 256 * u, row = e >> 3, seg = e & 7;
      st[4 + u] = ld_nt(a.w2 + (size_t)(56 * hi + (row < 56 ? row : 0)) * DM + 64 * cj + 8 * seg);
    }
#pragma unroll
    for (int i = 0; i < 4 * NG; ++i) kv[i] = ld_nt(a.kc + kvbase + (size_t)i * 4 * DH);
#pragma unroll
    for (int i = 0; i < 4 * VPG; ++i) vpre[i] = ld_nt(a.vc + kvbase + (size_t)i * 4 * DH);
    __builtin_amdgcn_sched_barrier(0);
#pragma unroll
    for (int u = 0; u < 4; ++u) {
      const int e = t4 + 256 * u, row = e >> 3, seg = e & 7;
      *reinterpret_cast<u32x4*>(lds + L_WO + row * WROW + 16 * seg) = st[u];
    }
#pragma unroll
    for (int u = 0; u < 2; ++u) {
      const int e = t4 + 256 * u, row = e >> 3, seg = e & 7;
      *reinterpret_cast<u32x4*>(lds + L_W2 + row * WROW + 16 * seg) = row < 56 ? st[4 + u] : u32x4{0u, 0u, 0u, 0u};
    }
    reinterpret_cast<u32*>(lds + L_H)[256 + t4] = 0u;
    reinterpret_cast<u32*>(lds + L_O)[512 + t4] = 0u;
    reinterpret_cast<u32*>(lds + L_O)[768 + t4] = 0u;
    asm volatile("s_waitcnt lgkmcnt(0)" ::: "memory");
    if (lane == 0) __hip_atomic_fetch_add(flags + 2, 1u, __ATOMIC_RELAXED, __HIP_MEMORY_SCOPE_WORKGROUP);
  }
  if (ROLE == 1) {
#pragma unroll
    for (int i = 0; i < 4 * NG; ++i) kv[i] = ld_nt(a.kc + kvbase + (size_t)i * 4 * DH);
#pragma unroll
    for (int i = 0; i < 4 * VPG; ++i) vpre[i] = ld_nt(a.vc + kvbase + (size_t)i * 4 * DH);
    __builtin_amdgcn_sched_barrier(0);
  }

  if (ROLE == 0) {
    lds_wait_ge(flags + 1, 3u);
    {
      const u64 e = epoch_raw + 1ull;
      tag.lo = (u32)__builtin_amdgcn_readfirstlane((u32)e);
      tag.hi = (u32)__builtin_amdgcn_readfirstlane((u32)(e >> 32));
      if (lane == 0) { flags[6] = tag.lo; flags[7] = tag.hi; }
    }
    const float* svc = reinterpret_cast<const float*>(lds + L_SVC);
    if (lane < 32) {
      const int b = lane >> 2, cq = lane & 3;
      float sv[4];
#pragma unroll
      for (int e = 0; e < 4; ++e) {
        float s0 = 0.f;
#pragma unroll
        for (int u = 0; u < 4; ++u) s0 += svc[u * 256 + b * 16 + 4 * cq + e];
        sv[e] = s0;
      }
      put_granule(rs, G_Q + b * 1024 + 4 * qb + cq, pack2(sv[0], sv[1]), pack2(sv[2], sv[3]), tag);
    }
    {
      const int b = lane >> 3, col = lane & 7;
      float s0 = 0.f;
#pragma unroll
      for (int u = 0; u < 4; ++u) s0 += svc[u * 256 + 128 + b * 16 + col];
      const float t = bf16r(bf16r(s0) + bfu((u16)b1v));
      const float inner = 0.7978845608028654f * (t + 0.044715f * t * t * t);
      const float hv = 0.5f * t * (1.0f + tanhf(inner));
      const float h1 = dpp<XOR1>(hv);
      const u32 pr = (lane & 1) ? pack2(h1, hv) : pack2(hv, h1);
      const u32 pr2 = __builtin_bit_cast(u32, dpp<XOR2>(__builtin_bit_cast(float, pr)));
      if (h_on && (lane & 3) == 0) put_granule(rs, G_H + b * 448 + 2 * qb + (col >> 2), pr, pr2, tag);
    }
    u32x4 gq[2], gh[2];
    for (u32 spins = 0; spins < SPIN_CAP; ++spins) {
      asm volatile("" ::: "memory");
      bool ok = true;
#pragma unroll
      for (int k = 0; k < 2; ++k) { gq[k] = get_granule(rs, G_Q + ab * 1024 + ahkv * 128 + 64 * k + lane); ok &= tag_ok(gq[k], tag); }
      if (__all(ok)) break;
      __builtin_amdgcn_s_sleep(1);
    }
    u64* qs = reinterpret_cast<u64*>(lds + L_Q);
#pragma unroll
    for (int k = 0; k < 2; ++k) qs[64 * k + lane] = ((u64)gq[k][1] << 32) | gq[k][0];
    asm volatile("s_waitcnt lgkmcnt(0)" ::: "memory");
    if (lane == 0) __hip_atomic_store(flags, 1u, __ATOMIC_RELAXED, __HIP_MEMORY_SCOPE_WORKGROUP);
#pragma unroll
    for (int i = 0; i < 4 * NG; ++i) kv[i] = ld_nt(a.kc + kvbase + (size_t)i * 4 * DH);
    u32 hg[2]; int hidx[2];
#pragma unroll
    for (int k = 0; k < 2; ++k) {
      const int idx = min(lane + 64 * k, 111);
      const int b = idx / 14, t = idx % 14;
      hg[k] = G_H + b * 448 + 14 * hi + t;
      hidx[k] = b * 16 + t;
    }
    for (u32 spins = 0; spins < SPIN_CAP; ++spins) {
      asm volatile("" ::: "memory");
      bool ok = true;
#pragma unroll
      for (int k = 0; k < 2; ++k) { gh[k] = get_granule(rs, hg[k]); ok &= tag_ok(gh[k], tag); }
      if (__all(ok)) break;
      __builtin_amdgcn_s_sleep(1);
    }
    u64* hs = reinterpret_cast<u64*>(lds + L_H);
#pragma unroll
    for (int k = 0; k < 2; ++k) if (lane + 64 * k < 112) hs[hidx[k]] = ((u64)gh[k][1] << 32) | gh[k][0];
    if (lane < 16) hs[(lane >> 1) * 16 + 14 + (lane & 1)] = 0ull;
    lds_wait_ge(flags + 2, 4u);
    asm volatile("s_waitcnt lgkmcnt(0)" ::: "memory");
#pragma unroll
    for (int ct = 0; ct < 4; ++ct) {
      f32x4 acc = {0.f, 0.f, 0.f, 0.f};
#pragma unroll
      for (int s = 0; s < 2; ++s) {
        const u32x4 af = *reinterpret_cast<const u32x4*>(lds + L_H + c * 128 + (32 * s + 8 * r) * 2);
        s16x8 bf;
#pragma unroll
        for (int jj = 0; jj < 8; ++jj)
          bf[jj] = *reinterpret_cast<const short*>(lds + L_W2 + (32 * s + 8 * r + jj) * WROW + (16 * ct + c) * 2);
        acc = __builtin_amdgcn_mfma_f32_16x16x32_bf16(__builtin_bit_cast(s16x8, af), bf, acc, 0, 0, 0);
      }
#pragma unroll
      for (int k = 0; k < 4; ++k) {
        const float nb = dpp<XOR1>(acc[k]);
        if (lane < 32 && (c & 1) == 0)
          put_granule(rs, G_Y + ((cj * NB + (4 * r + k)) * 32 + hi) * 32 + 8 * ct + (c >> 1), __float_as_uint(acc[k]), __float_as_uint(nb), tag);
      }
    }
  } else {
    __builtin_amdgcn_s_setprio(0);
    lds_wait_ge(flags, 1u);
  }

  s16x8 qf[4];
#pragma unroll
  for (int s = 0; s < 4; ++s) {
    u32x4 t = *reinterpret_cast<const u32x4*>(lds + L_Q + (c & 3) * 256 + 64 * s + 16 * r);
    if (c >= 4) t = u32x4{0u, 0u, 0u, 0u};
    qf[s] = __builtin_bit_cast(s16x8, t);
  }
  float sc[NG][4];
  unsigned char* ktile = lds + L_KT + v * 4352;
#pragma unroll
  for (int grp = 0; grp < NG; ++grp) {
#pragma unroll
    for (int t = 0; t < 4; ++t) *reinterpret_cast<u32x4*>(ktile + (4 * t + r) * 272 + c * 16) = kv[4 * grp + t];
    asm volatile("s_waitcnt lgkmcnt(0)" ::: "memory");
    u32x4 kf[4];
#pragma unroll
    for (int s = 0; s < 4; ++s) kf[s] = *reinterpret_cast<const u32x4*>(ktile + c * 272 + (4 * s + r) * 16);
    asm volatile("s_waitcnt lgkmcnt(0)" ::: "memory");
    f32x4 d = {0.f, 0.f, 0.f, 0.f};
#pragma unroll
    for (int s = 0; s < 4; ++s) d = __builtin_amdgcn_mfma_f32_16x16x32_bf16(__builtin_bit_cast(s16x8, kf[s]), qf[s], d, 0, 0, 0);
#pragma unroll
    for (int k = 0; k < 4; ++k) sc[grp][k] = bf16r(bf16r(d[k]) * 0.08838834764831845f);
  }
  float m = sc[0][0];
#pragma unroll
  for (int grp = 0; grp < NG; ++grp)
#pragma unroll
    for (int k = 0; k < 4; ++k) m = fmaxf(m, sc[grp][k]);
  m = xrow16_max(m);
  float l = 0.f;
  {
    float* pl = reinterpret_cast<float*>(lds + L_P) + v * 576;
#pragma unroll
    for (int grp = 0; grp < NG; ++grp)
#pragma unroll
      for (int k = 0; k < 4; ++k) {
        const float p = __builtin_amdgcn_exp2f((sc[grp][k] - m) * LOG2E);
        l += p;
        if (c < 4) pl[(16 * grp + 4 * r + k) * 4 + c] = p;
      }
  }
  l = xrow16_sum(l);
  asm volatile("s_waitcnt lgkmcnt(0)" ::: "memory");

  constexpr int VAH = 3;
  f32x2 acc2[16];
#pragma unroll
  for (int k = 0; k < 16; ++k) acc2[k] = f32x2{0.f, 0.f};
  {
    const f32x4* pl4 = reinterpret_cast<const f32x4*>(lds + L_P) + v * 144;
#pragma unroll
    for (int grp = 0; grp < NG; ++grp) {
#pragma unroll
      for (int gg = (grp == 0 ? VPG : VPG + VAH - 1 + grp); gg <= VPG + VAH - 1 + grp; ++gg) {
        if (gg < NG) {
#pragma unroll
          for (int t = 0; t < 4; ++t) kv[4 * (gg - VPG) + t] = ld_nt(a.vc + kvbase + (size_t)(4 * gg + t) * 4 * DH);
        }
      }
      __builtin_amdgcn_sched_barrier(0);
#pragma unroll
      for (int t = 0; t < 4; ++t) {
        const int i = 4 * grp + t;
        const f32x4 p = pl4[4 * i + r];
#pragma unroll
        for (int k = 0; k < 4; ++k) {
          const u32 vv = (i < 4 * VPG) ? vpre[i < 4 * VPG ? i : 0][k] : kv[i >= 4 * VPG ? i - 4 * VPG : 0][k];
          const f32x2 vf = {bflo(vv), bfhi(vv)};
#pragma unroll
          for (int g = 0; g < 4; ++g) acc2[g * 4 + k] = __builtin_elementwise_fma(f32x2{p[g], p[g]}, vf, acc2[g * 4 + k]);
        }
      }
      __builtin_amdgcn_sched_barrier(0);
    }
  }
  float acc[32];
#pragma unroll
  for (int k = 0; k < 32; ++k) acc[k] = xrow16_sum(acc2[k >> 1][k & 1]);
  {
    float* mrg = reinterpret_cast<float*>(lds + L_MRG) + v * 4 * 132;
    if (r == 0) {
#pragma unroll
      for (int g = 0; g < 4; ++g) {
        *reinterpret_cast<f32x4*>(mrg + g * 132 + 4 + 8 * c) = f32x4{acc[g * 8], acc[g * 8 + 1], acc[g * 8 + 2], acc[g * 8 + 3]};
        *reinterpret_cast<f32x4*>(mrg + g * 132 + 4 + 8 * c + 4) = f32x4{acc[g * 8 + 4], acc[g * 8 + 5], acc[g * 8 + 6], acc[g * 8 + 7]};
      }
      if (c < 4) { mrg[c * 132] = m; mrg[c * 132 + 1] = l; }
    }
  }
  if (ROLE == 0 && hi < 8) {
    const u32 yg = G_Y + (cj * NB + hi) * 32 * 32 + (lane & 31) + (lane >> 5) * 32;
    float y0 = 0.f, y1 = 0.f;
    for (u32 spins = 0; spins < SPIN_CAP; ++spins) {
      asm volatile("" ::: "memory");
      bool ok = true; y0 = 0.f; y1 = 0.f;
#pragma unroll
      for (int i = 0; i < 16; ++i) { const u32x4 g = get_granule(rs, yg + 64 * i); y0 += __uint_as_float(g[0]); y1 += __uint_as_float(g[1]); ok &= tag_ok(g, tag); }
      if (__all(ok)) break;
      __builtin_amdgcn_s_sleep(8);
    }
    {
      auto t0 = __builtin_amdgcn_permlane32_swap(__float_as_uint(y0), __float_as_uint(y0), false, false);
      y0 = __uint_as_float(t0[0]) + __uint_as_float(t0[1]);
      auto t1 = __builtin_amdgcn_permlane32_swap(__float_as_uint(y1), __float_as_uint(y1), false, false);
      y1 = __uint_as_float(t1[0]) + __uint_as_float(t1[1]);
    }
    if (lane < 32) {
      const u32 b2p = *reinterpret_cast<const u32*>(a.b2 + 64 * cj + 2 * lane);
      const float ya = bf16r(bf16r(y0) + bflo(b2p)), yb = bf16r(bf16r(y1) + bfhi(b2p));
      reinterpret_cast<float*>(lds + L_G)[2 * lane] = bf16r(ya / (1.0f + __expf(-ya)));
      reinterpret_cast<float*>(lds + L_G)[2 * lane + 1] = bf16r(yb / (1.0f + __expf(-yb)));
    }
  }
  wg_barrier();
  if (ROLE != 0) {
    tag.lo = (u32)__builtin_amdgcn_readfirstlane(flags[6]);
    tag.hi = (u32)__builtin_amdgcn_readfirstlane(flags[7]);
  }

  if (tid < 256) {
    const int g = tid >> 6, d2 = tid & 63;
    const float* mrg = reinterpret_cast<const float*>(lds + L_MRG) + g * 132;
    float M = mrg[0];
#pragma unroll
    for (int u = 1; u < 8; ++u) M = fmaxf(M, mrg[u * 4 * 132]);
    float L = 0.f, o0 = 0.f, o1 = 0.f;
#pragma unroll
    for (int u = 0; u < 8; ++u) {
      const float wt = __builtin_amdgcn_exp2f((mrg[u * 4 * 132] - M) * LOG2E);
      L = fmaf(wt, mrg[u * 4 * 132 + 1], L);
      o0 = fmaf(wt, mrg[u * 4 * 132 + 4 + 2 * d2], o0);
      o1 = fmaf(wt, mrg[u * 4 * 132 + 5 + 2 * d2], o1);
    }
    const u32 pg = G_P + ((ab * HQ + ahkv * 4 + g) * 4 + split) * 66;
    put_granule(rs, pg + d2, __float_as_uint(o0), __float_as_uint(o1), tag);
    if (d2 == 0) put_granule(rs, pg + 64, __float_as_uint(M), __float_as_uint(L), tag);
  }

  s16x8 wof[4];
  if (ROLE <= 1) {
#pragma unroll
    for (int s = 0; s < 4; ++s)
#pragma unroll
      for (int jj = 0; jj < 8; ++jj)
        wof[s][jj] = *reinterpret_cast<const short*>(lds + L_WO + (32 * s + 8 * r + jj) * WROW + (16 * v + c) * 2);
  }
  u32 xp = 0;
  if (ROLE == 2 && hi < 8 && v == 4) xp = *reinterpret_cast<const u32*>(a.x + hi * DM + 64 * cj + 2 * (lane & 31));
  {
    const u32 pg = G_P + (v * HQ + hi) * 4 * 66;
    probe_wait(rs, pg + (lane & 3) * 66 + 64, lane < 4, tag);
    u32x4 go[4], gm[4];
    for (u32 spins = 0; spins < SPIN_CAP; ++spins) {
      asm volatile("" ::: "memory");
      bool ok = true;
#pragma unroll
      for (int s = 0; s < 4; ++s) {
        go[s] = get_granule(rs, pg + s * 66 + lane);
        gm[s] = get_granule(rs, pg + s * 66 + 64);
        ok &= tag_ok(go[s], tag) & tag_ok(gm[s], tag);
      }
      if (__all(ok)) break;
      __builtin_amdgcn_s_sleep(2);
    }
    float M = __uint_as_float(gm[0][0]);
#pragma unroll
    for (int s = 1; s < 4; ++s) M = fmaxf(M, __uint_as_float(gm[s][0]));
    float L = 0.f, a0 = 0.f, a1 = 0.f;
#pragma unroll
    for (int s = 0; s < 4; ++s) {
      const float wt = __builtin_amdgcn_exp2f((__uint_as_float(gm[s][0]) - M) * LOG2E);
      L = fmaf(wt, __uint_as_float(gm[s][1]), L);
      a0 = fmaf(wt, __uint_as_float(go[s][0]), a0);
      a1 = fmaf(wt, __uint_as_float(go[s][1]), a1);
    }
    const float inv = 1.0f / L;
    reinterpret_cast<u32*>(lds + L_O)[v * 64 + lane] = pack2(a0 * inv, a1 * inv);
  }
  wg_barrier();
  if (ROLE <= 1) {
    const int ct = v;
    f32x4 acc4 = {0.f, 0.f, 0.f, 0.f};
#pragma unroll
    for (int s = 0; s < 4; ++s) {
      const u32x4 af = *reinterpret_cast<const u32x4*>(lds + L_O + c * 256 + (32 * s + 8 * r) * 2);
      acc4 = __builtin_amdgcn_mfma_f32_16x16x32_bf16(__builtin_bit_cast(s16x8, af), wof[s], acc4, 0, 0, 0);
    }
#pragma unroll
    for (int k = 0; k < 4; ++k) {
      const float nb = dpp<XOR1>(acc4[k]);
      if (lane < 32 && (c & 1) == 0)
        put_granule(rs, G_S + ((cj * NB + (4 * r + k)) * 32 + hi) * 32 + 8 * ct + (c >> 1), __float_as_uint(acc4[k]), __float_as_uint(nb), tag);
    }
  }
  if (ROLE == 2 && hi < 8 && v == 4) {
    const u32 sg = G_S + (cj * NB + hi) * 32 * 32;
    float s0 = 0.f, s1 = 0.f;
    for (u32 spins = 0; spins < SPIN_CAP; ++spins) {
      asm volatile("" ::: "memory");
      bool ok = true; s0 = 0.f; s1 = 0.f;
#pragma unroll
      for (int i = 0; i < 16; ++i) { const u32x4 g = get_granule(rs, sg + lane + 64 * i); s0 += __uint_as_float(g[0]); s1 += __uint_as_float(g[1]); ok &= tag_ok(g, tag); }
      if (__all(ok)) break;
      __builtin_amdgcn_s_sleep(2);
    }
    {
      auto t0 = __builtin_amdgcn_permlane32_swap(__float_as_uint(s0), __float_as_uint(s0), false, false);
      s0 = __uint_as_float(t0[0]) + __uint_as_float(t0[1]);
      auto t1 = __builtin_amdgcn_permlane32_swap(__float_as_uint(s1), __float_as_uint(s1), false, false);
      s1 = __uint_as_float(t1[0]) + __uint_as_float(t1[1]);
    }
    if (lane < 32) {
      const float g0 = reinterpret_cast<const float*>(lds + L_G)[2 * lane], g1 = reinterpret_cast<const float*>(lds + L_G)[2 * lane + 1];
      const float r0 = bflo(xp) + bf16r(bf16r(s0) * g0), r1 = bfhi(xp) + bf16r(bf16r(s1) * g1);
      *reinterpret_cast<u32*>(a.out + hi * DM + 64 * cj + 2 * lane) = pack2(r0, r1);
    }
    if (w == 0 && lane == 0)
      __hip_atomic_store(a.ws, ((u64)tag.hi << 32) | tag.lo, __ATOMIC_RELAXED, __HIP_MEMORY_SCOPE_AGENT);
  }
}

__global__ void __launch_bounds__(NTHR, 2) fused_decode_kernel(Params a) {
  __shared__ __attribute__((aligned(16))) unsigned char lds[L_END];
  const int v = __builtin_amdgcn_readfirstlane(threadIdx.x >> 6);
  if (v == 0) body<0>(a, lds, 0);
  else if (v < 4) body<1>(a, lds, v);
  else body<2>(a, lds, v);
}

extern "C" void kernel_launch(void* const* d_in, const int* in_sizes, int n_in,
                              void* d_out, int out_size, void* d_ws, size_t ws_size,
                              hipStream_t stream) {
  Params p;
  p.x = (const u16*)d_in[0];
  p.kc = (const u16*)d_in[1];
  p.vc = (const u16*)d_in[2];
  p.wq = (const u16*)d_in[3];
  p.wo = (const u16*)d_in[4];
  p.w1 = (const u16*)d_in[6];
  p.b1 = (const u16*)d_in[7];
  p.w2 = (const u16*)d_in[8];
  p.b2 = (const u16*)d_in[9];
  p.out = (u16*)d_out;
  p.ws = (u64*)d_ws;
  hipLaunchKernelGGL(fused_decode_kernel, dim3(NWG), dim3(NTHR), 0, stream, p);
}
```
